```python
import torch

def _softmax(x, axis=-1):
    return torch.softmax(x, dim=axis)
def _gelu(x):
    return torch.nn.functional.gelu(x, approximate="tanh")
def _silu(x):
    return torch.nn.functional.silu(x)
def _sigmoid(x):
    return torch.sigmoid(x)
def _layernorm(x, axis=-1, eps=1e-5):
    m = x.mean(dim=axis, keepdim=True)
    v = x.var(dim=axis, keepdim=True, unbiased=False)
    return (x - m) / torch.sqrt(v + eps)
def _scatter_add(tgt, idx, src, axis=0):
    return tgt.clone().index_add_(axis, idx, src)
def _segment_sum(src, seg, n):
    out = torch.zeros((n,) + src.shape[1:], dtype=src.dtype)
    return out.index_add_(0, seg, src)
def _pad(x, pw):
    flat = tuple(v for pair in reversed(pw) for v in pair)
    return torch.nn.functional.pad(x, flat)

kernel_name = 'gather_scatter__hybrid_graph_tf_nagphormer__graph'

def setup_inputs():

    _w = (lambda a: (lambda t: t.to(torch.bfloat16) if t.is_floating_point() else t)(torch.as_tensor(a)))
    N, deg, D = 10000, 32, 128
    E = N * deg
    src = (lambda lo, hi, *s: torch.randint(lo, hi, s))(0, N, E).to(torch.int64)
    dst = (lambda lo, hi, *s: torch.randint(lo, hi, s))(0, N, E).to(torch.int64)
    deg_cnt = torch.bincount(dst, minlength=N).to(float) + 1.0
    sc = 1.0 / D ** 0.5
    return {
        "x": _w((lambda *s: torch.randn(*s, dtype=torch.bfloat16) * 0.1)(N, D)),
        "src": _w(src),
        "dst": _w(dst),
        "deg_inv_sqrt": _w(1.0 / (lambda _x: torch.sqrt(torch.as_tensor(_x)))(deg_cnt)),
        "wq": _w((lambda *s: torch.randn(*s, dtype=torch.bfloat16) * 1.6)(D, D) * sc),
        "wk": _w((lambda *s: torch.randn(*s, dtype=torch.bfloat16) * 1.6)(D, D) * sc),
        "wv": _w((lambda *s: torch.randn(*s, dtype=torch.bfloat16) * 0.1)(D, D) * sc),
        "wo": _w((lambda *s: torch.randn(*s, dtype=torch.bfloat16) * 0.1)(D, D) * sc),
        "w_r": _w((lambda *s: torch.randn(*s, dtype=torch.bfloat16) * 0.1)(D)),
    }

def reference(x, src, dst, deg_inv_sqrt, wq, wk, wv, wo, w_r):
    N, D = x.shape[0], x.shape[1]
    K = 7
    h = x
    toks = [x]
    for _ in range(K):
        msg = h[src] * deg_inv_sqrt[src, None] * deg_inv_sqrt[dst, None]
        h = _segment_sum(msg, dst, N)
        toks.append(h)
    tok = torch.stack(toks, axis=1)
    t = _layernorm(tok, axis=-1)
    q   = torch.einsum('nkd,de->nke', t, wq)
    key = torch.einsum('nkd,de->nke', t, wk)
    v   = torch.einsum('nkd,de->nke', t, wv)
    scale = 1.0 / (D ** 0.5)
    attn = _softmax(torch.einsum('nkd,nld->nkl', q, key) * scale, axis=-1)
    o = torch.einsum('nkl,nld->nkd', attn, v)
    o = tok + torch.einsum('nkd,de->nke', o, wo)
    r = _layernorm(o, axis=-1)
    alpha = _softmax(torch.einsum('nkd,d->nk', r, w_r), axis=-1)
    return torch.einsum('nk,nkd->nd', alpha, o)
```

```cpp
#include <hip/hip_runtime.h>
#include <hip/hip_bf16.h>
#include <stdint.h>

#define NN 10000
#define DD 128
#define KH 7

typedef unsigned short u16;
__device__ __forceinline__ float bf2f(u16 v) { return __uint_as_float(((unsigned)v) << 16); }
__device__ __forceinline__ u16 f2bf(float f) { __hip_bfloat16 b = __float2bfloat16(f); return *(u16*)&b; }

__global__ void k_hist(const int* dst, int E, int* cnt) {
  int i = blockIdx.x * blockDim.x + threadIdx.x;
  if (i < E) atomicAdd(&cnt[dst[i]], 1);
}
__global__ void k_scan(const int* cnt, int* start, int* cur, int N) {
  __shared__ int s[1024];
  int t = threadIdx.x;
  int per = (N + 1023) / 1024;
  int sum = 0;
  for (int j = 0; j < per; j++) { int i = t * per + j; if (i < N) sum += cnt[i]; }
  s[t] = sum; __syncthreads();
  for (int o = 1; o < 1024; o <<= 1) { int v = t >= o ? s[t - o] : 0; __syncthreads(); s[t] += v; __syncthreads(); }
  int run = s[t] - sum;
  for (int j = 0; j < per; j++) { int i = t * per + j; if (i < N) { start[i] = run; cur[i] = run; run += cnt[i]; } }
  if (t == 1023) start[N] = s[1023];
}
__global__ void k_fill(const int* src, const int* dst, int E, int* cur, int* col) {
  int i = blockIdx.x * blockDim.x + threadIdx.x;
  if (i < E) { int p = atomicAdd(&cur[dst[i]], 1); col[p] = src[i]; }
}
__global__ void k_spmm(const u16* hin, u16* hout, const int* start, const int* col, const u16* dis, int N) {
  int w = (blockIdx.x * blockDim.x + threadIdx.x) >> 6, l = threadIdx.x & 63;
  if (w >= N) return;
  float a0 = 0, a1 = 0;
  for (int e = start[w]; e < start[w + 1]; e++) {
    int s = col[e];
    unsigned v = ((const unsigned*)hin)[s * 64 + l];
    float d = bf2f(dis[s]);
    a0 += bf2f(f2bf(bf2f(v & 0xffff) * d));
    a1 += bf2f(f2bf(bf2f(v >> 16) * d));
  }
  float dd = bf2f(dis[w]);
  ((unsigned*)hout)[w * 64 + l] = (unsigned)f2bf(a0 * dd) | ((unsigned)f2bf(a1 * dd) << 16);
}
__global__ void __launch_bounds__(128) k_attn(const u16* x, const u16* toks, const u16* wq, const u16* wk, const u16* wv, const u16* wo,
                       const u16* wr, u16* out, int N) {
  __shared__ float tok[8][DD], t[8][DD], q[8][DD], kk[8][DD], v[8][DD], o[8][DD], at[8][8], al[8], st[8][2];
  int n = blockIdx.x, d = threadIdx.x;
  for (int k = 0; k < 8; k++) tok[k][d] = bf2f(k == 0 ? x[n * DD + d] : toks[((size_t)(k - 1) * N + n) * DD + d]);
  __syncthreads();
  if (d < 8) { float m = 0; for (int j = 0; j < DD; j++) m += tok[d][j]; m /= DD; float vv = 0; for (int j = 0; j < DD; j++) { float z = tok[d][j] - m; vv += z * z; } vv /= DD; st[d][0] = m; st[d][1] = 1.f / sqrtf(vv + 1e-5f); }
  __syncthreads();
  for (int k = 0; k < 8; k++) t[k][d] = (tok[k][d] - st[k][0]) * st[k][1];
  __syncthreads();
  for (int k = 0; k < 8; k++) { float a = 0, b = 0, c = 0; for (int j = 0; j < DD; j++) { a += t[k][j] * bf2f(wq[j * DD + d]); b += t[k][j] * bf2f(wk[j * DD + d]); c += t[k][j] * bf2f(wv[j * DD + d]); } q[k][d] = a; kk[k][d] = b; v[k][d] = c; }
  __syncthreads();
  if (d < 64) { int a = d >> 3, b = d & 7; float s = 0; for (int j = 0; j < DD; j++) s += q[a][j] * kk[b][j]; at[a][b] = s * 0.08838834764831845f; }
  __syncthreads();
  if (d < 8) { float m = -1e30f; for (int j = 0; j < 8; j++) m = fmaxf(m, at[d][j]); float s = 0; for (int j = 0; j < 8; j++) { float e = __expf(at[d][j] - m); at[d][j] = e; s += e; } for (int j = 0; j < 8; j++) at[d][j] /= s; }
  __syncthreads();
  for (int k = 0; k < 8; k++) { float a = 0; for (int j = 0; j < 8; j++) a += at[k][j] * v[j][d]; q[k][d] = a; }
  __syncthreads();
  for (int k = 0; k < 8; k++) { float a = 0; for (int j = 0; j < DD; j++) a += q[k][j] * bf2f(wo[j * DD + d]); o[k][d] = tok[k][d] + a; }
  __syncthreads();
  if (d < 8) { float m = 0; for (int j = 0; j < DD; j++) m += o[d][j]; m /= DD; float vv = 0; for (int j = 0; j < DD; j++) { float z = o[d][j] - m; vv += z * z; } vv /= DD; float rs = 1.f / sqrtf(vv + 1e-5f); float s = 0; for (int j = 0; j < DD; j++) s += (o[d][j] - m) * rs * bf2f(wr[j]); al[d] = s; }
  __syncthreads();
  float m = -1e30f; for (int j = 0; j < 8; j++) m = fmaxf(m, al[j]);
  float s = 0, a = 0; for (int j = 0; j < 8; j++) { float e = __expf(al[j] - m); s += e; a += e * o[j][d]; }
  out[n * DD + d] = f2bf(a / s);
}

extern "C" void kernel_launch(void* const* d_in, const int* in_sizes, int n_in, void* d_out, int out_size, void* d_ws,
                              size_t ws_size, hipStream_t stream) {
  const u16* x = (const u16*)d_in[0]; const int* src = (const int*)d_in[1]; const int* dst = (const int*)d_in[2];
  const u16* dis = (const u16*)d_in[3]; const u16* wq = (const u16*)d_in[4]; const u16* wk = (const u16*)d_in[5];
  const u16* wv = (const u16*)d_in[6]; const u16* wo = (const u16*)d_in[7]; const u16* wr = (const u16*)d_in[8];
  int N = in_sizes[3], E = in_sizes[1];
  char* ws = (char*)d_ws;
  int* cnt = (int*)ws; int* start = cnt + 16384; int* cur = start + 16384; int* col = cur + 16384;
  u16* toks = (u16*)(ws + (8 << 20));
  hipMemsetAsync(cnt, 0, 16384 * 4, stream);
  k_hist<<<(E + 255) / 256, 256, 0, stream>>>(dst, E, cnt);
  k_scan<<<1, 1024, 0, stream>>>(cnt, start, cur, N);
  k_fill<<<(E + 255) / 256, 256, 0, stream>>>(src, dst, E, cur, col);
  for (int k = 0; k < KH; k++)
    k_spmm<<<(N * 64 + 255) / 256, 256, 0, stream>>>(k == 0 ? x : toks + (size_t)(k - 1) * N * DD, toks + (size_t)k * N * DD, start, col, dis, N);
  k_attn<<<N, 128, 0, stream>>>(x, toks, wq, wk, wv, wo, wr, (u16*)d_out, N);
}
```

```cpp
#include <hip/hip_runtime.h>
#include <stdint.h>

typedef unsigned short u16;
typedef unsigned u32;
typedef u32 u32x2 __attribute__((ext_vector_type(2)));
typedef u32 u32x4 __attribute__((ext_vector_type(4)));
typedef u32 u32x8 __attribute__((ext_vector_type(8)));
typedef float f32x2 __attribute__((ext_vector_type(2)));
typedef float f32x4 __attribute__((ext_vector_type(4)));
typedef float f32x16 __attribute__((ext_vector_type(16)));
typedef __bf16 bf16x2 __attribute__((ext_vector_type(2)));
typedef __bf16 bf16x8 __attribute__((ext_vector_type(8)));
#define LAS __attribute__((address_space(3)))
#define GAS __attribute__((address_space(1)))

constexpr int NN = 10000, EE = 320000, NB = 125, TSTR = 81920, TB = 80000;
constexpr int NW1 = 128, EPW = 2500, CAP1 = 48;
constexpr size_t O_G = 0;
constexpr size_t O_H = O_G + (size_t)7 * 32 * TSTR;
constexpr size_t O_CELL = O_H + (size_t)8 * 32 * TSTR;
constexpr size_t O_OV1C = O_CELL + (size_t)NB * NW1 * CAP1 * 4;
constexpr size_t O_OV1 = O_OV1C + 512;
constexpr size_t O_IMG = O_OV1 + (size_t)EE * 4;
constexpr size_t O_META = O_IMG + (size_t)NB * 24 * 256;
constexpr size_t O_OV2C = O_META + (size_t)NB * 512;
constexpr size_t O_W = O_OV2C + 1024;
constexpr size_t O_OV2 = O_W + 65536;

__device__ __forceinline__ u32 pk_bf16(float a, float b) {
  f32x2 v = {a, b};
  return __builtin_bit_cast(u32, __builtin_convertvector(v, bf16x2));
}
__device__ __forceinline__ float bflo(u32 w) { return __uint_as_float(w << 16); }
__device__ __forceinline__ float bfhi(u32 w) { return __uint_as_float(w & 0xffff0000u); }
__device__ __forceinline__ f32x16 mfma32(u32x4 a, u32x4 b, f32x16 c) {
  return __builtin_amdgcn_mfma_f32_32x32x16_bf16(__builtin_bit_cast(bf16x8, a), __builtin_bit_cast(bf16x8, b), c, 0, 0, 0);
}
template <int CTRL> __device__ __forceinline__ u32 dpp(u32 v) { return __builtin_amdgcn_update_dpp(0u, v, CTRL, 0xf, 0xf, true); }
template <int CTRL> __device__ __forceinline__ float dppf(float v) { return __uint_as_float(dpp<CTRL>(__float_as_uint(v))); }
__device__ __forceinline__ float xor32f(float v) {
  auto r = __builtin_amdgcn_permlane32_swap(__float_as_uint(v), __float_as_uint(v), false, false);
  return __uint_as_float((threadIdx.x & 32) ? r[0] : r[1]);
}
__device__ __forceinline__ int ffbl(u32 v) { int r; asm("v_ffbl_b32 %0, %1" : "=v"(r) : "v"(v)); return r; }
__device__ __forceinline__ u32x4 pack8(const f32x16& a, int o) {
  return (u32x4){pk_bf16(a[o], a[o + 1]), pk_bf16(a[o + 2], a[o + 3]), pk_bf16(a[o + 4], a[o + 5]), pk_bf16(a[o + 6], a[o + 7])};
}

__global__ __launch_bounds__(1024) void k_prep(const u16* __restrict__ x, const int* __restrict__ src, const int* __restrict__ dst,
                                               const u16* __restrict__ dis, const u16* __restrict__ wq, const u16* __restrict__ wk,
                                               const u16* __restrict__ wv, const u16* __restrict__ wo, char* __restrict__ ws) {
  __shared__ __attribute__((aligned(16))) char lds[2 * 32 * 656];
  const int tid = threadIdx.x, b = blockIdx.x;
  if (b < NW1) {
    u32* img = (u32*)lds;
    u32* cnt = img + NB * CAP1;
    u32x4 s4 = {0, 0, 0, 0}, d4 = {0, 0, 0, 0};
    if (tid < EPW / 4) { s4 = ((const u32x4*)src)[b * (EPW / 4) + tid]; d4 = ((const u32x4*)dst)[b * (EPW / 4) + tid]; }
    const u32x4 inv = {~0u, ~0u, ~0u, ~0u};
    ((u32x4*)img)[tid] = inv;
    if (tid < 1500 - 1024) ((u32x4*)img)[1024 + tid] = inv;
    if (tid < 128) cnt[tid] = 0;
    __syncthreads();
    if (tid < EPW / 4) {
      u32 r[4], bk[4];
#pragma unroll
      for (int i = 0; i < 4; i++) { bk[i] = (d4[i] * 52429u) >> 22; r[i] = atomicAdd(&cnt[bk[i]], 1u); }
#pragma unroll
      for (int i = 0; i < 4; i++) {
        if (r[i] < CAP1) img[bk[i] * CAP1 + (r[i] & 7) * 6 + (r[i] >> 3)] = s4[i] | ((d4[i] - bk[i] * 80) << 16);
        else { u32 o = atomicAdd(&cnt[127], 1u); ((u32*)(ws + O_OV1))[b * EPW + o] = s4[i] | (d4[i] << 14); }
      }
    }
    __syncthreads();
#pragma unroll
    for (int it = 0; it < 2; it++) {
      const u32 i = it * 1024 + tid;
      if (i < 1500) { const u32 c = i / 12, p = i - c * 12; *(u32x4*)(ws + O_CELL + (size_t)(c * NW1 + b) * 192 + p * 16) = ((u32x4*)img)[i]; }
    }
    if (tid == 0) ((u32*)(ws + O_OV1C))[b] = cnt[127];
  } else if (b < NW1 + NB) {
    const int t = b - NW1;
    u32x4 v[2]; float dn[2];
#pragma unroll
    for (int it = 0; it < 2; it++) {
      const u32 i = it * 1024 + tid;
      if (i < 1280) { v[it] = ((const u32x4*)x)[t * 1280 + i]; dn[it] = __uint_as_float((u32)dis[t * 80 + (i >> 4)] << 16); }
    }
#pragma unroll
    for (int it = 0; it < 2; it++) {
      const u32 i = it * 1024 + tid;
      if (i < 1280) {
        const u32 sl = i / 40, o = i - sl * 40;
        *(u32x4*)(ws + O_G + (size_t)(32 + sl) * TSTR + t * 640 + o * 16) = (u32x4){~0u, ~0u, ~0u, ~0u};
        char* p = lds + (i & 15) * 1312 + (i >> 4) * 8;
        *(u32x2*)p = (u32x2){v[it].x, v[it].y};
        *(u32x2*)(p + 656) = (u32x2){v[it].z, v[it].w};
        u32 g[4];
#pragma unroll
        for (int q = 0; q < 4; q++) g[q] = pk_bf16(bflo(v[it][q]) * dn[it], bfhi(v[it][q]) * dn[it]);
        *(u32x2*)(p + 32 * 656) = (u32x2){g[0], g[1]};
        *(u32x2*)(p + 33 * 656) = (u32x2){g[2], g[3]};
      }
    }
    __syncthreads();
#pragma unroll
    for (int it = 0; it < 2; it++) {
      const u32 i = it * 1024 + tid;
      if (i < 1280) {
        const u32 sl = i / 40, o = i - sl * 40;
        *(u32x4*)(ws + O_G + (size_t)sl * TSTR + t * 640 + o * 16) = *(const u32x4*)(lds + (32 + sl) * 656 + o * 16);
        *(u32x4*)(ws + O_H + (size_t)sl * TSTR + t * 640 + o * 16) = *(const u32x4*)(lds + sl * 656 + o * 16);
      }
    }
  } else {
    const bool isM = b == NW1 + NB;
    const int lane = tid & 63, w = tid >> 6, r = lane & 31, h = lane >> 5, db = w & 3, cb = w >> 2;
    float* pc = (float*)lds;
    float* pd = pc + 1024;
    float* md = pd + 1024;
    f32x16 acc = {}, act = {};
    if (isM) {
#pragma unroll
      for (int j = 0; j < 8; j++) {
        const u32x4 a = *(const u32x4*)(wq + (db * 32 + r) * 128 + j * 16 + h * 8);
        const u32x4 k = *(const u32x4*)(wk + (cb * 32 + r) * 128 + j * 16 + h * 8);
        acc = mfma32(a, k, acc);
        act = mfma32(k, a, act);
      }
    } else {
#pragma unroll
      for (int eb = 0; eb < 4; eb++) {
        f32x16 t = {};
#pragma unroll
        for (int jj = 0; jj < 2; jj++) {
          const u32x4 a = *(const u32x4*)(wo + (eb * 32 + r) * 128 + cb * 32 + jj * 16 + h * 8);
          const int k = r - jj * 16 - h * 8;
          u32x4 id;
#pragma unroll
          for (int q = 0; q < 4; q++) id[q] = k == 2 * q ? 0x3f80u : k == 2 * q + 1 ? 0x3f800000u : 0u;
          t = mfma32(a, id, t);
        }
#pragma unroll
        for (int jj = 0; jj < 2; jj++) {
          const u16* p = wv + (db * 32 + r) * 128 + eb * 32 + jj * 16 + h * 4;
          const u32x2 lo = *(const u32x2*)p, hi = *(const u32x2*)(p + 8);
          acc = mfma32((u32x4){lo.x, lo.y, hi.x, hi.y}, pack8(t, jj * 8), acc);
        }
      }
    }
    float s = 0, st = 0;
#pragma unroll
    for (int i = 0; i < 16; i++) { s += acc[i]; st += act[i]; }
    pc[(db * 2 + h) * 128 + cb * 32 + r] = s;
    pd[(cb * 2 + h) * 128 + db * 32 + r] = st;
    __syncthreads();
    if (tid < 128) { float a = 0;
#pragma unroll
      for (int i = 0; i < 8; i++) a += pd[i * 128 + tid];
      md[tid] = a * (1.f / 128); }
    float mc = 0;
#pragma unroll
    for (int i = 0; i < 8; i++) mc += pc[i * 128 + cb * 32 + r];
    mc *= 1.f / 128;
    __syncthreads();
    float tm = md[lane] + md[lane + 64];
#pragma unroll
    for (int o = 32; o; o >>= 1) tm += __shfl_xor(tm, o);
    tm *= 1.f / 128;
    const float sc = isM ? 0.08838834764831845f * 1.4426950408889634f : 1.f;
#pragma unroll
    for (int i = 0; i < 16; i++) acc[i] = (acc[i] - mc - md[db * 32 + (i & 3) + 8 * (i >> 2) + 4 * h] + tm) * sc;
    char* o = ws + O_W + (isM ? 0 : 32768) + ((cb * 8 + db * 2) * 64 + lane) * 16;
    *(u32x4*)o = pack8(acc, 0);
    *(u32x4*)(o + 1024) = pack8(acc, 8);
  }
}

template <int R> struct SchedRound {
  static __device__ __forceinline__ void run(const u32* E, u32& F, u32& L, u32 obase) {
    SchedRound<R - 1>::run(E, F, L, obase);
    constexpr int r = R - 1;
    if (r > 0) {
      constexpr int z = __builtin_ctz(r | 32);
      if (z == 0) F = dpp<0xB1>(F);
      else if (z == 1) F = dpp<0x4E>(F);
      else if (z == 2) F = dpp<0x141>(F);
      else if (z == 3) F = dpp<0x128>(F);
      else F = __builtin_amdgcn_ds_swizzle(F, 0x401F);
    }
    const u32 w = E[r >> 1];
    const u32 e = (r & 1) ? w >> 16 : w & 0xffffu;
    const u32 has = min(e, 1u);
    const int i = max(ffbl(F & L), ffbl(L));
    const u32 bit = has << (i & 31);
    F &= ~bit;
    L &= ~(bit | __builtin_bitreverse32(bit));
    *(LAS u16*)(uintptr_t)(obase + 2 * i) = (u16)e;
  }
};
template <> struct SchedRound<0> { static __device__ __forceinline__ void run(const u32*, u32&, u32&, u32) {} };

__global__ __launch_bounds__(1024) void k_sched(const u16* __restrict__ dis, char* __restrict__ ws) {
  constexpr int ES = 72;
  __shared__ __attribute__((aligned(16))) char zer[10240 + 2 * 192 * ES + 768 + 16];
  __shared__ __attribute__((aligned(16))) char e2[192 * ES];
  __shared__ __attribute__((aligned(16))) u16 info[2560];
  __shared__ __attribute__((aligned(16))) u16 key[80];
  __shared__ unsigned char p2n[80];
  u32* cnt = (u32*)zer;
  char* e1 = zer + 10240;
  char* o2 = e1 + 192 * ES;
  u32* m2 = (u32*)(o2 + 192 * ES);
  u32* ovc = m2 + 192;
  const int tid = threadIdx.x, lane = tid & 63, wave = __builtin_amdgcn_readfirstlane(tid >> 6);
  const int b = blockIdx.x >> 1, part = blockIdx.x & 1;
  u32 en[6];
  {
    const u32x2* p = (const u32x2*)(ws + O_CELL + (size_t)b * NW1 * 192 + tid * 24);
    const u32x2 a0 = p[0], a1 = p[1], a2 = p[2];
    en[0] = a0.x; en[1] = a0.y; en[2] = a1.x; en[3] = a1.y; en[4] = a2.x; en[5] = a2.y;
  }
  u32 c1 = 0;
  if (tid < NW1) c1 = ((const u32*)(ws + O_OV1C))[tid];
  constexpr int NZ = sizeof(zer) / 16;
#pragma unroll
  for (int it = 0; it < (NZ + 1023) / 1024; it++) if (it * 1024 + tid < NZ) ((u32x4*)zer)[it * 1024 + tid] = (u32x4){0, 0, 0, 0};
  __syncthreads();
  if (c1) ovc[1] = 1;
#pragma unroll
  for (int i = 0; i < 6; i++) {
    if (en[i] != ~0u) {
      const u32 cell = (en[i] >> 16) * 32 + (en[i] & 31);
      const u32 r = atomicAdd(&cnt[cell], 1u);
      en[i] = (en[i] & 0x3fff) | (cell << 14) | (min(r, 8u) << 26);
    }
  }
  __syncthreads();
  u32 pre[4];
  const int node = tid >> 3, q8 = tid & 7;
  if (tid < 640) {
    const u32x4 c = ((const u32x4*)cnt)[tid];
    const u32 c0 = min(c.x, 8u), cc1 = min(c.y, 8u), c2 = min(c.z, 8u), c3 = min(c.w, 8u);
    const u32 tot = c0 + cc1 + c2 + c3;
    u32 s = tot, t;
    t = dpp<0x111>(s); s += q8 >= 1 ? t : 0;
    t = dpp<0x112>(s); s += q8 >= 2 ? t : 0;
    t = dpp<0x114>(s); s += q8 >= 4 ? t : 0;
    pre[0] = s - tot; pre[1] = pre[0] + c0; pre[2] = pre[1] + cc1; pre[3] = pre[2] + c2;
    if (q8 == 7) key[node] = (u16)((s << 7) | (127 - node));
  }
  __syncthreads();
  if (tid < 640) {
    const u32 mine = key[node];
    const u32* kp = (const u32*)key + q8 * 5;
    u32 n = 0;
#pragma unroll
    for (int i = 0; i < 5; i++) { const u32 k2 = kp[i]; n += ((k2 & 0xffff) > mine) + ((k2 >> 16) > mine); }
    n += dpp<0xB1>(n); n += dpp<0x4E>(n); n += dpp<0x141>(n);
    const u32 ps = n << 8;
    ((u32x2*)info)[tid] = (u32x2){(pre[0] | ps) | ((pre[1] | ps) << 16), (pre[2] | ps) | ((pre[3] | ps) << 16)};
    if (q8 == 0) p2n[n] = (unsigned char)node;
  }
  __syncthreads();
  u32* ov2 = (u32*)(ws + O_OV2) + (size_t)b * EE;
#pragma unroll
  for (int i = 0; i < 6; i++) {
    if (en[i] != ~0u) {
      const u32 cell = (en[i] >> 14) & 0xfff, r = en[i] >> 26, val = (en[i] & 0x3fff) + 32;
      const u32 inf = info[cell], pos = inf >> 8, tile = pos >> 4, col = pos & 15, p = (inf & 255) + r;
      const u32 tl = tile - 3 * part;
      if (tl < 3u - part) {
        if (r >= 8 || p >= 48 - 8 * ((tile + 1) >> 1)) {
          const u32 o = atomicAdd(&ovc[0], 1u);
          ov2[part ? EE - 1 - o : o] = val | ((cell >> 5) << 16);
        } else {
          const u32 q = (p + col) & 3, ln = col + ((q & 1) << 5) + ((q & 2) << 3);
          const u32 xx = (cell ^ ln) & 31, g = xx ^ (((xx >> 2) & 1) * 3);
          u32 rr = g ^ (g >> 1); rr ^= rr >> 2; rr ^= rr >> 4;
          const u32 row = tl * 64 + ln;
          if (r < 4) *(u16*)(e1 + row * ES + 8 + rr * 2) = (u16)val;
          else { *(u16*)(e2 + row * ES + 8 + rr * 2) = (u16)val; atomicOr(&m2[row], 1u << rr); }
        }
      }
    }
  }
  if (part == 0 && ovc[1]) {
    for (int w = 0; w < NW1; w++) {
      const u32 n = ((const u32*)(ws + O_OV1C))[w];
      for (u32 i = tid; i < n; i += 1024) {
        const u32 e = ((const u32*)(ws + O_OV1))[w * EPW + i], d = e >> 14;
        if (d - b * 80 < 80u) { const u32 o = atomicAdd(&ovc[0], 1u); ov2[o] = ((e & 0x3fff) + 32) | ((d - b * 80) << 16); }
      }
    }
  }
  __syncthreads();
  if (wave < 3 - part) {
    const int tile = wave + 3 * part, S = 6 - ((tile + 1) >> 1), P = tile == 0 ? 0 : tile == 4 ? 20 : 5 * tile + 1;
    const u32 row = wave * 64 + lane, hh = lane >> 5;
    u32 E[16];
#pragma unroll
    for (int i = 0; i < 8; i++) { const u32x2 t = *(const u32x2*)(e1 + row * ES + 8 + i * 8); E[2 * i] = t.x; E[2 * i + 1] = t.y; }
    const u32 full = (1u << (2 * S)) - 1;
    u32 L = full | __builtin_bitreverse32(full);
    u32 F = L & ~(((lane ^ hh) & 1) ? 0u : 1u << ((lane & 31) >> 1));
    const u32 ob = (u32)(uintptr_t)(LAS char*)(o2 + row * ES + 8);
    SchedRound<32>::run(E, F, L, ob);
    u32 m = m2[row];
    while (m) {
      const int rr = ffbl(m);
      m &= m - 1;
      const int i = 16 + ffbl(L >> 16);
      const u32 bit = 1u << i;
      L &= ~(bit | __builtin_bitreverse32(bit));
      *(LAS u16*)(uintptr_t)(ob + 2 * i) = *(const u16*)(e2 + row * ES + 8 + rr * 2);
    }
    u32* img = (u32*)(ws + O_IMG) + (b * 24 + P) * 64 + lane;
#pragma unroll
    for (int s2 = 0; s2 < 3; s2++) {
      const u32x2 a = *(LAS const u32x2*)(uintptr_t)(ob + s2 * 8), c = *(LAS const u32x2*)(uintptr_t)(ob + 56 - s2 * 8);
#pragma unroll
      for (int k = 0; k < 2; k++) {
        const int s = s2 * 2 + k;
        const u32 hi = k ? c.x : c.y;
        const u32 w = (k ? a.y : a.x) | ((hi >> 16) | (hi << 16));
        const u32 z = (4 * s + hh) * 0x10001u + 0x20000u;
        const u32 o = max(w & 0xffffu, z & 0xffffu) | (max(w >> 16, z >> 16) << 16);
        if (s < S) img[s * 64] = o;
      }
    }
  } else if (wave == 3) {
    if (part == 0) {
      const u32 n0 = p2n[lane], n1 = p2n[64 + (lane & 15)];
      ((u32x2*)(ws + O_META))[b * 64 + lane] = (u32x2){n0 | ((u32)dis[b * 80 + n0] << 16), n1 | ((u32)dis[b * 80 + n1] << 16)};
    }
    if (lane == 0) ((u32*)(ws + O_OV2C))[b * 2 + part] = ovc[0];
  }
}

#define S_(x) #x
#define S(x) S_(x)
#define RS(p) "88+4*((" S(p) ")%%4)"
#define LD(p) \
  ".if " S(p) " < 24\n" \
  "v_lshlrev_b32_sdwa v104, 3, v[64+" S(p) "] dst_sel:DWORD dst_unused:UNUSED_PAD src0_sel:DWORD src1_sel:WORD_0\n" \
  "v_lshlrev_b32_sdwa v105, 3, v[64+" S(p) "] dst_sel:DWORD dst_unused:UNUSED_PAD src0_sel:DWORD src1_sel:WORD_1\n" \
  "ds_read_b64 v[" RS(p) ":" RS(p) "+1], v104\n" \
  "ds_read_b64 v[" RS(p) "+2:" RS(p) "+3], v105\n" \
  ".endif\n"
#define MF(p) \
  ".if (" S(p) ") == 6 || (" S(p) ") == 11 || (" S(p) ") == 16 || (" S(p) ") == 20\n" \
  "v_mov_b32_dpp v60, v60 row_ror:4 row_mask:0xf bank_mask:0xf\n" \
  "v_mov_b32_dpp v61, v61 row_ror:4 row_mask:0xf bank_mask:0xf\n" \
  "v_mov_b32_dpp v62, v62 row_ror:4 row_mask:0xf bank_mask:0xf\n" \
  "v_mov_b32_dpp v63, v63 row_ror:4 row_mask:0xf bank_mask:0xf\n" \
  "s_nop 1\n .endif\n" \
  ".if (" S(p) ") == 0\n v_mfma_f32_16x16x32_bf16 v[52:55], v[60:63], v[" RS(p) ":" RS(p) "+3], 0\n" \
  ".elseif (" S(p) ") < 20\n v_mfma_f32_16x16x32_bf16 v[52:55], v[60:63], v[" RS(p) ":" RS(p) "+3], v[52:55]\n" \
  ".elseif (" S(p) ") == 20\n v_mfma_f32_16x16x32_bf16 v[56:59], v[60:63], v[" RS(p) ":" RS(p) "+3], 0\n" \
  ".else\n v_mfma_f32_16x16x32_bf16 v[56:59], v[60:63], v[" RS(p) ":" RS(p) "+3], v[56:59]\n .endif\n"
#define PS(p) \
  ".if " S(p) " == 6 || " S(p) " == 12 || " S(p) " == 18\n s_setprio 3-" S(p) "/6\n .endif\n" \
  LD(p+3) \
  ".if " S(p) " < 22\n s_waitcnt lgkmcnt(4)\n .else\n s_waitcnt lgkmcnt(0)\n .endif\n" \
  MF(p) LD(p+4) MF(p+1)

__device__ __forceinline__ void gather(const u32x8* ix, u32x4& A, f32x4& a0, f32x4& a1) {
  asm volatile(
      "s_waitcnt lgkmcnt(0)\n"
      "s_setprio 3\n"
      LD(0) LD(1) LD(2)
      PS(0) PS(2) PS(4) PS(6) PS(8) PS(10) PS(12) PS(14) PS(16) PS(18) PS(20) PS(22)
      "s_setprio 0\n"
      "s_nop 7\n"
      : "={v[52:55]}"(a0), "={v[56:59]}"(a1), "+{v[60:63]}"(A)
      : "{v[64:71]}"(ix[0]), "{v[72:79]}"(ix[1]), "{v[80:87]}"(ix[2])
      : "memory", "v88", "v89", "v90", "v91", "v92", "v93", "v94", "v95", "v96", "v97", "v98", "v99", "v100", "v101", "v102", "v103",
        "v104", "v105");
}

constexpr int LDS_HOPS = 256 + TB + 16 * 1280;
__global__ __launch_bounds__(1024, 4) void k_hops(char* __restrict__ ws) {
  extern __shared__ __attribute__((aligned(16))) char lds[];
  const int tid = threadIdx.x, lane = tid & 63, wave = __builtin_amdgcn_readfirstlane(tid >> 6);
  const int b = blockIdx.x, xg = b & 7, j = b >> 3, slice = 4 * xg + (j & 3), range = j >> 2;
  const int bucket = range * 16 + wave;
  const bool active = bucket < NB;
  const __amdgpu_buffer_rsrc_t rsG = __builtin_amdgcn_make_buffer_rsrc(ws + O_G, 0, 7 * 32 * TSTR, 0x00020000);
  const __amdgpu_buffer_rsrc_t rsH = __builtin_amdgcn_make_buffer_rsrc(ws + O_H, 0, 8 * 32 * TSTR, 0x00020000);
  const bool last = tid < (TB - 4 * 16384) / 16;
  const u32 lo = tid * 16, lo4 = last ? lo + 65536 : 0x80000000u;
  char* tab = lds + 256;
  u32x4 v[5];
#pragma unroll
  for (int i = 0; i < 5; i++) v[i] = __builtin_amdgcn_raw_buffer_load_b128(rsG, i < 4 ? lo + i * 16384 : lo4, slice * TSTR, 0);
  const int bk = active ? bucket : 0;
  u32x8 ix[3];
#pragma unroll
  for (int t = 0; t < 3; t++)
#pragma unroll
    for (int s = 0; s < 8; s++) ix[t][s] = ((const u32*)(ws + O_IMG))[(bk * 24 + t * 8 + s) * 64 + lane];
  const u32x2 mt = ((const u32x2*)(ws + O_META))[bk * 64 + lane];
  const u32x2 oc = ((const u32x2*)(ws + O_OV2C))[bk];
  const u32 c0 = __builtin_amdgcn_readfirstlane(oc.x), nov = c0 + __builtin_amdgcn_readfirstlane(oc.y);
  const u32* ov2 = (const u32*)(ws + O_OV2) + (size_t)bk * EE;
  u32 ove = 0;
  if (lane < nov) ove = ov2[lane < c0 ? lane : EE - 1 - (lane - c0)];
  const float dis0 = bfhi(mt.x), dis1 = bfhi(mt.y);
  const u32 n0 = mt.x & 0xffff, n1 = mt.y & 0xffff;
  u32x4 A;
  {
    const int r = lane & 15;
    const u32 one = (r >> 2) ? 0u : (r & 1) ? 0x3f800000u : 0x00003f80u;
    A.x = A.z = (r & 2) ? 0u : one;
    A.y = A.w = (r & 2) ? one : 0u;
  }
  char* stage = lds + 256 + TB + wave * 1280;
  if (tid < 16) *(u32x4*)(lds + tid * 16) = (u32x4){0, 0, 0, 0};
  u32x4 hprev = {0, 0, 0, 0};
  const u32 reg = slice * TSTR + bucket * 640 + lane * 16;
  const bool wr = active && lane < 40;
#pragma unroll 1
  for (int k = 1; k <= 7; k++) {
    {
      const u32 tb = ((k - 1) * 32 + slice) * TSTR;
      u32 pend = 0;
#pragma unroll
      for (int i = 0; i < 5; i++) {
        const u32 m = max(max(v[i].x, v[i].y), max(v[i].z, v[i].w));
        if (i < 4 || last) *(u32x4*)(tab + lo + i * 16384) = v[i];
        if (__any(m == ~0u)) pend |= 1u << i;
      }
      if (k == 1) pend = 0;
      u32 spins = 0;
      while (pend) {
        asm volatile("" ::: "memory");
        const int i = __builtin_ctz(pend);
        const u32x4 w = __builtin_amdgcn_raw_buffer_load_b128(rsG, i < 4 ? lo + i * 16384 : lo4, tb, 16);
        const u32 m = max(max(w.x, w.y), max(w.z, w.w));
        if (!__any(m == ~0u)) { pend &= pend - 1; if (i < 4 || last) *(u32x4*)(tab + lo + i * 16384) = w; }
        if (++spins > 4000000u) break;
      }
    }
    __syncthreads();
    if (wr) {
      if (k <= 5) { u32 m1 = ~0u; asm volatile("" : "+v"(m1)); __builtin_amdgcn_raw_buffer_store_b128((u32x4){m1, m1, m1, m1}, rsG, reg, (k + 1) * 32 * TSTR, 16); }
      if (k > 1) __builtin_amdgcn_raw_buffer_store_b128(hprev, rsH, reg, (k - 1) * 32 * TSTR, 0);
    }
    if (active) {
      if (nov) {
        *(u32x4*)(stage + lane * 16) = (u32x4){0, 0, 0, 0};
        if (lane < 16) *(u32x4*)(stage + 1024 + lane * 16) = (u32x4){0, 0, 0, 0};
        u32 e = ove;
        for (u32 base = 0;;) {
          const u32x2 row = *(const u32x2*)(lds + (e & 0xffff) * 8);
          LAS float* p = (LAS float*)(stage + (e >> 16) * 16);
          __builtin_amdgcn_ds_faddf(p, bflo(row.x), 0, 0, false);
          __builtin_amdgcn_ds_faddf(p + 1, bfhi(row.x), 0, 0, false);
          __builtin_amdgcn_ds_faddf(p + 2, bflo(row.y), 0, 0, false);
          __builtin_amdgcn_ds_faddf(p + 3, bfhi(row.y), 0, 0, false);
          base += 64;
          if (base >= nov) break;
          const u32 i = base + lane;
          e = i < nov ? ov2[i < c0 ? i : EE - 1 - (i - c0)] : 0u;
        }
      }
      f32x4 acc0, acc1;
      gather(ix, A, acc0, acc1);
      if (nov) {
        acc0 += *(const f32x4*)(stage + n0 * 16);
        acc1 += *(const f32x4*)(stage + n1 * 16);
        asm volatile("s_waitcnt lgkmcnt(0)" ::: "memory");
      }
      const u32x2 h0 = {pk_bf16(acc0[0] * dis0, acc0[1] * dis0), pk_bf16(acc0[2] * dis0, acc0[3] * dis0)};
      const u32x2 h1 = {pk_bf16(acc1[0] * dis1, acc1[1] * dis1), pk_bf16(acc1[2] * dis1, acc1[3] * dis1)};
      u32x2 g0 = {pk_bf16(bflo(h0.x) * dis0, bfhi(h0.x) * dis0), pk_bf16(bflo(h0.y) * dis0, bfhi(h0.y) * dis0)};
      u32x2 g1 = {pk_bf16(bflo(h1.x) * dis1, bfhi(h1.x) * dis1), pk_bf16(bflo(h1.y) * dis1, bfhi(h1.y) * dis1)};
      g0.x = min(g0.x, 0xfffffffeu); g0.y = min(g0.y, 0xfffffffeu); g1.x = min(g1.x, 0xfffffffeu); g1.y = min(g1.y, 0xfffffffeu);
      *(u32x2*)(stage + n0 * 8) = g0;
      *(u32x2*)(stage + 640 + n0 * 8) = h0;
      if (lane < 16) { *(u32x2*)(stage + n1 * 8) = g1; *(u32x2*)(stage + 640 + n1 * 8) = h1; }
      __builtin_amdgcn_wave_barrier();
      const int l40 = lane < 40 ? lane : 0;
      const u32x4 gv = *(const u32x4*)(stage + l40 * 16);
      hprev = *(const u32x4*)(stage + 640 + l40 * 16);
      asm volatile("s_waitcnt vmcnt(0)" ::: "memory");
      if (lane < 40) {
        if (k <= 6) __builtin_amdgcn_raw_buffer_store_b128(gv, rsG, reg, k * 32 * TSTR, 16);
        else __builtin_amdgcn_raw_buffer_store_b128(hprev, rsH, reg, 7 * 32 * TSTR, 0);
      }
    }
    if (k == 7) break;
    asm volatile("s_waitcnt vmcnt(0)" ::: "memory");
    __syncthreads();
    asm volatile("" ::: "memory");
#pragma unroll
    for (int i = 0; i < 5; i++) v[i] = __builtin_amdgcn_raw_buffer_load_b128(rsG, i < 4 ? lo + i * 16384 : lo4, (k * 32 + slice) * TSTR, 16);
  }
}

constexpr int HS = 10240 + 32;
constexpr int LDS_ATTN = 65536 + 8 * HS + 512;
__global__ __launch_bounds__(640) void k_attn(const char* __restrict__ ws, const u16* __restrict__ wr, u16* __restrict__ out) {
  extern __shared__ __attribute__((aligned(16))) char lds[];
  const int tid = threadIdx.x, lane = tid & 63, wave = __builtin_amdgcn_readfirstlane(tid >> 6);
  const int nb = blockIdx.x * 40;
  char* tk = lds + 65536;
  float* wc = (float*)(tk + 8 * HS);
  {
    const u32 sl = tid / 20, o = tid - sl * 20;
    const char* g = ws + O_H + (size_t)sl * TSTR + nb * 8 + o * 16;
#pragma unroll
    for (int k = 0; k < 8; k++)
      __builtin_amdgcn_global_load_lds((const GAS void*)(g + (size_t)k * 32 * TSTR), (LAS void*)(tk + k * HS + wave * 1024), 16, 0, 0);
#pragma unroll
    for (int i = 0; i < 7; i++) {
      const int c = i * 10 + wave;
      if (c < 64) __builtin_amdgcn_global_load_lds((const GAS void*)(ws + O_W + c * 1024 + lane * 16), (LAS void*)(lds + c * 1024), 16, 0, 0);
    }
  }
  if (wave == 0) {
    const u32 w2 = ((const u32*)wr)[lane];
    float s = bflo(w2) + bfhi(w2);
#pragma unroll
    for (int o = 32; o; o >>= 1) s += __shfl_xor(s, o);
    s *= 1.f / 128;
    const int f = lane * 2, p = ((f >> 2) & 1) * 64 + (f >> 3) * 4 + (f & 3);
    wc[p] = (bflo(w2) - s) * 1.4426950408889634f;
    wc[p + 1] = (bfhi(w2) - s) * 1.4426950408889634f;
  }
  asm volatile("s_waitcnt vmcnt(0)" ::: "memory");
  __syncthreads();
  const int r = lane & 31, h = lane >> 5, m = r & 3, hop = r >> 2;
  u32x4 T[8];
  {
    const char* p = tk + hop * HS + h * 320 + (wave * 4 + m) * 8;
#pragma unroll
    for (int j = 0; j < 8; j++) {
      const u32x2 a = *(const u32x2*)(p + j * 4 * 320), c = *(const u32x2*)(p + (j * 4 + 2) * 320);
      T[j] = (u32x4){a.x, a.y, c.x, c.y};
    }
  }
  float rs;
  {
    float s = 0, q = 0;
    const bf16x2 ones = {(__bf16)1.f, (__bf16)1.f};
#pragma unroll
    for (int j = 0; j < 8; j++)
#pragma unroll
      for (int i = 0; i < 4; i++) {
        const u32 tw = T[j][i];
        const bf16x2 t = __builtin_bit_cast(bf16x2, tw);
        s = __builtin_amdgcn_fdot2_f32_bf16(t, ones, s, false);
        q = __builtin_amdgcn_fdot2_f32_bf16(t, t, q, false);
      }
    s += xor32f(s); q += xor32f(q);
    const float mu = s * (1.f / 128);
    rs = __builtin_amdgcn_rsqf(fmaxf(q * (1.f / 128) - mu * mu, 0.f) + 1e-5f);
  }
  f32x16 sa = {};
#pragma unroll
  for (int bb = 0; bb < 4; bb++) {
    f32x16 u = {};
#pragma unroll
    for (int j = 0; j < 8; j++) u = mfma32(*(const u32x4*)(lds + ((bb * 8 + j) * 64 + lane) * 16), T[j], u);
    sa = mfma32(T[2 * bb], pack8(u, 0), sa);
    sa = mfma32(T[2 * bb + 1], pack8(u, 8), sa);
  }
  u32x4 P0, P1;
  {
    float s[4], rl[4];
#pragma unroll
    for (int i = 0; i < 4; i++) {
      rl[i] = __uint_as_float(__builtin_amdgcn_ds_bpermute(4 * (m + 8 * i + 4 * h), __float_as_uint(rs)));
      const float lo = (m & 1) ? sa[4 * i + 1] : sa[4 * i], hi = (m & 1) ? sa[4 * i + 3] : sa[4 * i + 2];
      s[i] = ((m & 2) ? hi : lo) * rs * rl[i];
    }
    float mx = fmaxf(fmaxf(s[0], s[1]), fmaxf(s[2], s[3]));
    mx = fmaxf(mx, xor32f(mx));
    float sum = 0;
#pragma unroll
    for (int i = 0; i < 4; i++) { s[i] = __builtin_amdgcn_exp2f(s[i] - mx); sum += s[i]; }
    sum += xor32f(sum);
    const float inv = __builtin_amdgcn_rcpf(sum);
    const u32 mk = (m & 1) ? 0xffff0000u : 0xffffu, mA = (m & 2) ? 0u : mk, mB = (m & 2) ? mk : 0u;
    u32 pk[4];
#pragma unroll
    for (int i = 0; i < 4; i++) { const float p = s[i] * inv * rl[i]; pk[i] = pk_bf16(p, p); }
    P0 = (u32x4){pk[0] & mA, pk[0] & mB, pk[1] & mA, pk[1] & mB};
    P1 = (u32x4){pk[2] & mA, pk[2] & mB, pk[3] & mA, pk[3] & mB};
  }
  f32x16 o[4];
  f32x2 s1 = {0, 0}, s2 = {0, 0}, sd = {0, 0};
#pragma unroll
  for (int bb = 0; bb < 4; bb++) {
    f32x16 vv = {};
#pragma unroll
    for (int j = 0; j < 8; j++) vv = mfma32(T[j], *(const u32x4*)(lds + 32768 + ((bb * 8 + j) * 64 + lane) * 16), vv);
    f32x16 c;
#pragma unroll
    for (int i = 0; i < 8; i++) { const u32 t = T[2 * bb + (i >> 2)][i & 3]; c[2 * i] = bflo(t); c[2 * i + 1] = bfhi(t); }
    c = mfma32(pack8(vv, 0), P0, c);
    c = mfma32(pack8(vv, 8), P1, c);
    o[bb] = c;
#pragma unroll
    for (int i = 0; i < 4; i++) {
      const f32x4 w = *(const f32x4*)(wc + h * 64 + bb * 16 + i * 4);
      const f32x2 x0 = {c[4 * i], c[4 * i + 1]}, x1 = {c[4 * i + 2], c[4 * i + 3]};
      s1 += x0; s1 += x1;
      s2 += x0 * x0; s2 += x1 * x1;
      sd += x0 * (f32x2){w.x, w.y}; sd += x1 * (f32x2){w.z, w.w};
    }
  }
  float al;
  {
    float a = s1.x + s1.y, q = s2.x + s2.y, d = sd.x + sd.y;
    a += xor32f(a); q += xor32f(q); d += xor32f(d);
    const float mu = a * (1.f / 128);
    const float z = d * __builtin_amdgcn_rsqf(fmaxf(q * (1.f / 128) - mu * mu, 0.f) + 1e-5f);
    float mx = fmaxf(z, dppf<0x124>(z));
    mx = fmaxf(mx, dppf<0x128>(mx));
    { auto t = __builtin_amdgcn_permlane16_swap(__float_as_uint(mx), __float_as_uint(mx), false, false); mx = fmaxf(__uint_as_float(t[0]), __uint_as_float(t[1])); }
    const float e = __builtin_amdgcn_exp2f(z - mx);
    float sum = e + dppf<0x124>(e);
    sum += dppf<0x128>(sum);
    { auto t = __builtin_amdgcn_permlane16_swap(__float_as_uint(sum), __float_as_uint(sum), false, false); sum = __uint_as_float(t[0]) + __uint_as_float(t[1]); }
    al = e * __builtin_amdgcn_rcpf(sum);
  }
  float ae, ao;
  { auto t = __builtin_amdgcn_permlane16_swap(__float_as_uint(al), __float_as_uint(al), false, false); ae = __uint_as_float(t[0]); ao = __uint_as_float(t[1]); }
  float y[32];
#pragma unroll
  for (int g = 0; g < 2; g++)
#pragma unroll
    for (int i = 0; i < 16; i++) {
      auto t = __builtin_amdgcn_permlane16_swap(__float_as_uint(o[2 * g][i]), __float_as_uint(o[2 * g + 1][i]), false, false);
      y[g * 16 + i] = __uint_as_float(t[0]) * ae + __uint_as_float(t[1]) * ao;
    }
  float y2[16], y3[8];
#pragma unroll
  for (int g = 0; g < 2; g++)
#pragma unroll
    for (int i = 0; i < 8; i++) {
      const float a = y[g * 16 + i], c = y[g * 16 + 8 + i];
      const float a2 = a + dppf<0x128>(a), c2 = c + dppf<0x128>(c);
      y2[g * 8 + i] = (lane & 8) ? c2 : a2;
    }
#pragma unroll
  for (int g = 0; g < 2; g++)
#pragma unroll
    for (int i = 0; i < 4; i++) {
      const float a = y2[g * 8 + i], c = y2[g * 8 + 4 + i];
      const float a2 = a + dppf<0x104>(a), c2 = c + dppf<0x114>(c);
      y3[g * 4 + i] = (lane & 4) ? c2 : a2;
    }
  u16* op = out + (size_t)(nb + wave * 4 + m) * 128 + ((lane >> 4) & 1) * 32 + ((lane >> 3) & 1) * 16 + ((lane >> 2) & 1) * 8 + h * 4;
  *(u32x2*)op = (u32x2){pk_bf16(y3[0], y3[1]), pk_bf16(y3[2], y3[3])};
  *(u32x2*)(op + 64) = (u32x2){pk_bf16(y3[4], y3[5]), pk_bf16(y3[6], y3[7])};
}

extern "C" void kernel_launch(void* const* d_in, const int* in_sizes, int n_in, void* d_out, int out_size, void* d_ws, size_t ws_size,
                              hipStream_t stream) {
  char* ws = (char*)d_ws;
  (void)hipFuncSetAttribute((const void*)k_hops, hipFuncAttributeMaxDynamicSharedMemorySize, LDS_HOPS);
  (void)hipFuncSetAttribute((const void*)k_attn, hipFuncAttributeMaxDynamicSharedMemorySize, LDS_ATTN);
  k_prep<<<NW1 + NB + 2, 1024, 0, stream>>>((const u16*)d_in[0], (const int*)d_in[1], (const int*)d_in[2], (const u16*)d_in[3],
                                           (const u16*)d_in[4], (const u16*)d_in[5], (const u16*)d_in[6], (const u16*)d_in[7], ws);
  k_sched<<<2 * NB, 1024, 0, stream>>>((const u16*)d_in[3], ws);
  k_hops<<<256, 1024, LDS_HOPS, stream>>>(ws);
  k_attn<<<NN / 40, 640, LDS_ATTN, stream>>>(ws, (const u16*)d_in[8], (u16*)d_out);
}
```

```cpp
#include <hip/hip_runtime.h>
#include <stdint.h>

typedef unsigned short u16;
typedef unsigned u32;
typedef u32 u32x2 __attribute__((ext_vector_type(2)));
typedef u32 u32x4 __attribute__((ext_vector_type(4)));
typedef u32 u32x8 __attribute__((ext_vector_type(8)));
typedef float f32x2 __attribute__((ext_vector_type(2)));
typedef float f32x4 __attribute__((ext_vector_type(4)));
typedef float f32x16 __attribute__((ext_vector_type(16)));
typedef __bf16 bf16x2 __attribute__((ext_vector_type(2)));
typedef __bf16 bf16x8 __attribute__((ext_vector_type(8)));
#define LAS __attribute__((address_space(3)))
#define GAS __attribute__((address_space(1)))

constexpr int NN = 10000, EE = 320000, NB = 125, TSTR = 81920, TB = 80000;
constexpr int NW1 = 128, EPW = 2500, CAP1 = 48;
constexpr size_t O_G = 0;
constexpr size_t O_H = O_G + (size_t)7 * 32 * TSTR;
constexpr size_t O_CELL = O_H + (size_t)8 * 32 * TSTR;
constexpr size_t O_OV1C = O_CELL + (size_t)NB * NW1 * CAP1 * 4;
constexpr size_t O_OV1 = O_OV1C + 512;
constexpr size_t O_IMG = O_OV1 + (size_t)EE * 4;
constexpr size_t O_META = O_IMG + (size_t)NB * 24 * 256;
constexpr size_t O_OV2C = O_META + (size_t)NB * 512;
constexpr size_t O_W = O_OV2C + 1024;
constexpr size_t O_OV2 = O_W + 65536;

#define STMP(K, i)
__device__ __forceinline__ u32 pk_bf16(float a, float b) {
  f32x2 v = {a, b};
  return __builtin_bit_cast(u32, __builtin_convertvector(v, bf16x2));
}
__device__ __forceinline__ float bflo(u32 w) { return __uint_as_float(w << 16); }
__device__ __forceinline__ float bfhi(u32 w) { return __uint_as_float(w & 0xffff0000u); }
__device__ __forceinline__ f32x16 mfma32(u32x4 a, u32x4 b, f32x16 c) {
  return __builtin_amdgcn_mfma_f32_32x32x16_bf16(__builtin_bit_cast(bf16x8, a), __builtin_bit_cast(bf16x8, b), c, 0, 0, 0);
}
template <int CTRL> __device__ __forceinline__ u32 dpp(u32 v) { return __builtin_amdgcn_update_dpp(0u, v, CTRL, 0xf, 0xf, true); }
template <int CTRL> __device__ __forceinline__ float dppf(float v) { return __uint_as_float(dpp<CTRL>(__float_as_uint(v))); }
__device__ __forceinline__ float xor32f(float v) {
  auto r = __builtin_amdgcn_permlane32_swap(__float_as_uint(v), __float_as_uint(v), false, false);
  return __uint_as_float((threadIdx.x & 32) ? r[0] : r[1]);
}
__device__ __forceinline__ int ffbl(u32 v) { int r; asm("v_ffbl_b32 %0, %1" : "=v"(r) : "v"(v)); return r; }
__device__ __forceinline__ u32x4 pack8(const f32x16& a, int o) {
  return (u32x4){pk_bf16(a[o], a[o + 1]), pk_bf16(a[o + 2], a[o + 3]), pk_bf16(a[o + 4], a[o + 5]), pk_bf16(a[o + 6], a[o + 7])};
}

__global__ __launch_bounds__(1024) void k_prep(const u16* __restrict__ x, const int* __restrict__ src, const int* __restrict__ dst,
                                               const u16* __restrict__ dis, char* __restrict__ ws) {
  __shared__ __attribute__((aligned(16))) char lds[2 * 32 * 656];
  const int tid = threadIdx.x, b = blockIdx.x;
  STMP(0, 0);
  if (b < NW1) {
    u32* img = (u32*)lds;
    u32* cnt = img + NB * CAP1;
    u32x4 s4 = {0, 0, 0, 0}, d4 = {0, 0, 0, 0};
    if (tid < EPW / 4) { s4 = ((const u32x4*)src)[b * (EPW / 4) + tid]; d4 = ((const u32x4*)dst)[b * (EPW / 4) + tid]; }
    const u32x4 inv = {~0u, ~0u, ~0u, ~0u};
    ((u32x4*)img)[tid] = inv;
    if (tid < 1500 - 1024) ((u32x4*)img)[1024 + tid] = inv;
    if (tid < 128) cnt[tid] = 0;
    __syncthreads();
    STMP(0, 1);
    if (tid < EPW / 4) {
      u32 r[4], bk[4];
#pragma unroll
      for (int i = 0; i < 4; i++) { bk[i] = (d4[i] * 52429u) >> 22; r[i] = atomicAdd(&cnt[bk[i]], 1u); }
#pragma unroll
      for (int i = 0; i < 4; i++) {
        if (r[i] < CAP1) img[bk[i] * CAP1 + (r[i] & 7) * 6 + (r[i] >> 3)] = s4[i] | ((d4[i] - bk[i] * 80) << 16);
        else { u32 o = atomicAdd(&cnt[127], 1u); ((u32*)(ws + O_OV1))[b * EPW + o] = s4[i] | (d4[i] << 14); }
      }
    }
    STMP(0, 2);
    __syncthreads();
    STMP(0, 3);
#pragma unroll
    for (int it = 0; it < 2; it++) {
      const u32 i = it * 1024 + tid;
      if (i < 1500) { const u32 c = i / 12, p = i - c * 12; *(u32x4*)(ws + O_CELL + (size_t)(c * NW1 + b) * 192 + p * 16) = ((u32x4*)img)[i]; }
    }
    if (tid == 0) ((u32*)(ws + O_OV1C))[b] = cnt[127];
  } else {
    const int t = b - NW1;
    u32x4 v[2]; float dn[2];
#pragma unroll
    for (int it = 0; it < 2; it++) {
      const u32 i = it * 1024 + tid;
      if (i < 1280) { v[it] = ((const u32x4*)x)[t * 1280 + i]; dn[it] = __uint_as_float((u32)dis[t * 80 + (i >> 4)] << 16); }
    }
#pragma unroll
    for (int it = 0; it < 2; it++) {
      const u32 i = it * 1024 + tid;
      if (i < 1280) {
        const u32 sl = i / 40, o = i - sl * 40;
        *(u32x4*)(ws + O_G + (size_t)(32 + sl) * TSTR + t * 640 + o * 16) = (u32x4){~0u, ~0u, ~0u, ~0u};
        char* p = lds + (i & 15) * 1312 + (i >> 4) * 8;
        *(u32x2*)p = (u32x2){v[it].x, v[it].y};
        *(u32x2*)(p + 656) = (u32x2){v[it].z, v[it].w};
        u32 g[4];
#pragma unroll
        for (int q = 0; q < 4; q++) g[q] = pk_bf16(bflo(v[it][q]) * dn[it], bfhi(v[it][q]) * dn[it]);
        *(u32x2*)(p + 32 * 656) = (u32x2){g[0], g[1]};
        *(u32x2*)(p + 33 * 656) = (u32x2){g[2], g[3]};
      }
    }
    __syncthreads();
#pragma unroll
    for (int it = 0; it < 2; it++) {
      const u32 i = it * 1024 + tid;
      if (i < 1280) {
        const u32 sl = i / 40, o = i - sl * 40;
        *(u32x4*)(ws + O_G + (size_t)sl * TSTR + t * 640 + o * 16) = *(const u32x4*)(lds + (32 + sl) * 656 + o * 16);
        *(u32x4*)(ws + O_H + (size_t)sl * TSTR + t * 640 + o * 16) = *(const u32x4*)(lds + sl * 656 + o * 16);
      }
    }
  }
  STMP(0, 4);
  STMP(0, 5);
}

__device__ __forceinline__ void fold_weights(const u16* __restrict__ wa, const u16* __restrict__ wb, const bool isM, char* lds, char* __restrict__ ws) {
  const int tid = threadIdx.x, lane = tid & 63, w = tid >> 6, r = lane & 31, h = lane >> 5, db = w & 3, cb = w >> 2;
  constexpr int RS = 272, MB = 128 * RS;
  u32x4 t4[4];
#pragma unroll
  for (int i = 0; i < 4; i++) t4[i] = ((const u32x4*)(i < 2 ? wa : wb))[(i & 1) * 1024 + tid];
#pragma unroll
  for (int i = 0; i < 4; i++) *(u32x4*)(lds + (i >> 1) * MB + ((i & 1) * 64 + (tid >> 4)) * RS + (tid & 15) * 16) = t4[i];
  __syncthreads();
  f32x16 acc = {}, act = {};
  if (isM) {
#pragma unroll
    for (int j = 0; j < 8; j++) {
      const u32x4 a = *(const u32x4*)(lds + (db * 32 + r) * RS + j * 32 + h * 16);
      const u32x4 k = *(const u32x4*)(lds + MB + (cb * 32 + r) * RS + j * 32 + h * 16);
      acc = mfma32(a, k, acc);
      act = mfma32(k, a, act);
    }
  } else {
#pragma unroll
    for (int eb = 0; eb < 4; eb++) {
      f32x16 t = {};
#pragma unroll
      for (int jj = 0; jj < 2; jj++) {
        const u32x4 a = *(const u32x4*)(lds + MB + (eb * 32 + r) * RS + cb * 64 + jj * 32 + h * 16);
        const int k = r - jj * 16 - h * 8;
        u32x4 id;
#pragma unroll
        for (int q = 0; q < 4; q++) id[q] = k == 2 * q ? 0x3f80u : k == 2 * q + 1 ? 0x3f800000u : 0u;
        t = mfma32(a, id, t);
      }
#pragma unroll
      for (int jj = 0; jj < 2; jj++) {
        const char* p = lds + (db * 32 + r) * RS + eb * 64 + jj * 32 + h * 8;
        const u32x2 lo = *(const u32x2*)p, hi = *(const u32x2*)(p + 16);
        acc = mfma32((u32x4){lo.x, lo.y, hi.x, hi.y}, pack8(t, jj * 8), acc);
      }
    }
  }
  __syncthreads();
  float* pc = (float*)lds;
  float* pd = pc + 1024;
  float* md = pd + 1024;
  float s = 0, st = 0;
#pragma unroll
  for (int i = 0; i < 16; i++) { s += acc[i]; st += act[i]; }
  pc[(db * 2 + h) * 128 + cb * 32 + r] = s;
  pd[(cb * 2 + h) * 128 + db * 32 + r] = st;
  __syncthreads();
  if (tid < 128) { float a = 0;
#pragma unroll
    for (int i = 0; i < 8; i++) a += pd[i * 128 + tid];
    md[tid] = a * (1.f / 128); }
  float mc = 0;
#pragma unroll
  for (int i = 0; i < 8; i++) mc += pc[i * 128 + cb * 32 + r];
  mc *= 1.f / 128;
  __syncthreads();
  float tm = md[lane] + md[lane + 64];
#pragma unroll
  for (int o = 32; o; o >>= 1) tm += __shfl_xor(tm, o);
  tm *= 1.f / 128;
  const float sc = isM ? 0.08838834764831845f * 1.4426950408889634f : 1.f;
#pragma unroll
  for (int i = 0; i < 16; i++) acc[i] = (acc[i] - mc - md[db * 32 + (i & 3) + 8 * (i >> 2) + 4 * h] + tm) * sc;
  char* o = ws + O_W + (isM ? 0 : 32768) + ((cb * 8 + db * 2) * 64 + lane) * 16;
  *(u32x4*)o = pack8(acc, 0);
  *(u32x4*)(o + 1024) = pack8(acc, 8);
}

template <int R> struct SchedRound {
  static __device__ __forceinline__ void run(const u32* E, u32& F, u32& L, u32 obase) {
    SchedRound<R - 1>::run(E, F, L, obase);
    constexpr int r = R - 1;
    if (r > 0) {
      constexpr int z = __builtin_ctz(r | 32);
      if (z == 0) F = dpp<0xB1>(F);
      else if (z == 1) F = dpp<0x4E>(F);
      else if (z == 2) F = dpp<0x141>(F);
      else if (z == 3) F = dpp<0x128>(F);
      else F = __builtin_amdgcn_ds_swizzle(F, 0x401F);
    }
    const u32 w = E[r >> 1];
    const u32 e = (r & 1) ? w >> 16 : w & 0xffffu;
    const u32 has = min(e, 1u);
    const int i = max(ffbl(F & L), ffbl(L));
    const u32 bit = has << (i & 31);
    F &= ~bit;
    L &= ~(bit | __builtin_bitreverse32(bit));
    *(LAS u16*)(uintptr_t)(obase + 2 * i) = (u16)e;
  }
};
template <> struct SchedRound<0> { static __device__ __forceinline__ void run(const u32*, u32&, u32&, u32) {} };

__global__ __launch_bounds__(1024) void k_sched(const u16* __restrict__ dis, const u16* __restrict__ wq, const u16* __restrict__ wk,
                                                const u16* __restrict__ wv, const u16* __restrict__ wo, char* __restrict__ ws) {
  constexpr int ES = 72;
  constexpr int ZB = 10240 + 2 * 192 * ES + 768 + 16;
  __shared__ __attribute__((aligned(16))) char lds[2 * 128 * 272];
  if (blockIdx.x >= 2 * NB) {
    STMP(1, 0);
    if (blockIdx.x == 2 * NB) fold_weights(wq, wk, true, lds, ws); else fold_weights(wv, wo, false, lds, ws);
    STMP(1, 11);
    return;
  }
  char* zer = lds;
  char* e2 = lds + ZB;
  u16* info = (u16*)(e2 + 192 * ES);
  u16* key = info + 2560;
  unsigned char* p2n = (unsigned char*)(key + 80);
  u32* cnt = (u32*)zer;
  char* e1 = zer + 10240;
  char* o2 = e1 + 192 * ES;
  u32* m2 = (u32*)(o2 + 192 * ES);
  u32* ovc = m2 + 192;
  const int tid = threadIdx.x, lane = tid & 63, wave = __builtin_amdgcn_readfirstlane(tid >> 6);
  const int b = blockIdx.x >> 1, part = blockIdx.x & 1;
  STMP(1, 0);
  u32 en[6];
  {
    const u32x2* p = (const u32x2*)(ws + O_CELL + (size_t)b * NW1 * 192 + tid * 24);
    const u32x2 a0 = p[0], a1 = p[1], a2 = p[2];
    en[0] = a0.x; en[1] = a0.y; en[2] = a1.x; en[3] = a1.y; en[4] = a2.x; en[5] = a2.y;
  }
  u32 c1 = 0;
  if (tid < NW1) c1 = ((const u32*)(ws + O_OV1C))[tid];
  constexpr int NZ = ZB / 16;
#pragma unroll
  for (int it = 0; it < (NZ + 1023) / 1024; it++) if (it * 1024 + tid < NZ) ((u32x4*)zer)[it * 1024 + tid] = (u32x4){0, 0, 0, 0};
  __syncthreads();
  STMP(1, 1);
  if (c1) ovc[1] = 1;
#pragma unroll
  for (int i = 0; i < 6; i++) {
    if (en[i] != ~0u) {
      const u32 cell = (en[i] >> 16) * 32 + (en[i] & 31);
      const u32 r = atomicAdd(&cnt[cell], 1u);
      en[i] = (en[i] & 0x3fff) | (cell << 14) | (min(r, 8u) << 26);
    }
  }
  STMP(1, 2);
  __syncthreads();
  STMP(1, 3);
  u32 pre[4];
  const int node = tid >> 3, q8 = tid & 7;
  if (tid < 640) {
    const u32x4 c = ((const u32x4*)cnt)[tid];
    const u32 c0 = min(c.x, 8u), cc1 = min(c.y, 8u), c2 = min(c.z, 8u), c3 = min(c.w, 8u);
    const u32 tot = c0 + cc1 + c2 + c3;
    u32 s = tot, t;
    t = dpp<0x111>(s); s += q8 >= 1 ? t : 0;
    t = dpp<0x112>(s); s += q8 >= 2 ? t : 0;
    t = dpp<0x114>(s); s += q8 >= 4 ? t : 0;
    pre[0] = s - tot; pre[1] = pre[0] + c0; pre[2] = pre[1] + cc1; pre[3] = pre[2] + c2;
    if (q8 == 7) key[node] = (u16)((s << 7) | (127 - node));
  }
  __syncthreads();
  if (tid < 640) {
    const u32 mine = key[node];
    const u32* kp = (const u32*)key + q8 * 5;
    u32 n = 0;
#pragma unroll
    for (int i = 0; i < 5; i++) { const u32 k2 = kp[i]; n += ((k2 & 0xffff) > mine) + ((k2 >> 16) > mine); }
    n += dpp<0xB1>(n); n += dpp<0x4E>(n); n += dpp<0x141>(n);
    const u32 ps = n << 8;
    ((u32x2*)info)[tid] = (u32x2){(pre[0] | ps) | ((pre[1] | ps) << 16), (pre[2] | ps) | ((pre[3] | ps) << 16)};
    if (q8 == 0) p2n[n] = (unsigned char)node;
  }
  __syncthreads();
  STMP(1, 4);
  u32* ov2 = (u32*)(ws + O_OV2) + (size_t)b * EE;
#pragma unroll
  for (int i = 0; i < 6; i++) {
    if (en[i] != ~0u) {
      const u32 cell = (en[i] >> 14) & 0xfff, r = en[i] >> 26, val = (en[i] & 0x3fff) + 32;
      const u32 inf = info[cell], pos = inf >> 8, tile = pos >> 4, col = pos & 15, p = (inf & 255) + r;
      const u32 tl = tile - 3 * part;
      if (tl < 3u - part) {
        if (r >= 8 || p >= 48 - 8 * ((tile + 1) >> 1)) {
          const u32 o = atomicAdd(&ovc[0], 1u);
          ov2[part ? EE - 1 - o : o] = val | ((cell >> 5) << 16);
        } else {
          const u32 q = (p + col) & 3, ln = col + ((q & 1) << 5) + ((q & 2) << 3);
          const u32 xx = (cell ^ ln) & 31, g = xx ^ (((xx >> 2) & 1) * 3);
          u32 rr = g ^ (g >> 1); rr ^= rr >> 2; rr ^= rr >> 4;
          const u32 row = tl * 64 + ln;
          if (r < 4) *(u16*)(e1 + row * ES + 8 + rr * 2) = (u16)val;
          else { *(u16*)(e2 + row * ES + 8 + rr * 2) = (u16)val; atomicOr(&m2[row], 1u << rr); }
        }
      }
    }
  }
  if (part == 0 && ovc[1]) {
    for (int w = 0; w < NW1; w++) {
      const u32 n = ((const u32*)(ws + O_OV1C))[w];
      for (u32 i = tid; i < n; i += 1024) {
        const u32 e = ((const u32*)(ws + O_OV1))[w * EPW + i], d = e >> 14;
        if (d - b * 80 < 80u) { const u32 o = atomicAdd(&ovc[0], 1u); ov2[o] = ((e & 0x3fff) + 32) | ((d - b * 80) << 16); }
      }
    }
  }
  STMP(1, 5);
  __syncthreads();
  STMP(1, 6);
  if (wave < 3 - part) {
    const int tile = wave + 3 * part, S = 6 - ((tile + 1) >> 1), P = tile == 0 ? 0 : tile == 4 ? 20 : 5 * tile + 1;
    const u32 row = wave * 64 + lane, hh = lane >> 5;
    u32 E[16];
#pragma unroll
    for (int i = 0; i < 8; i++) { const u32x2 t = *(const u32x2*)(e1 + row * ES + 8 + i * 8); E[2 * i] = t.x; E[2 * i + 1] = t.y; }
    const u32 full = (1u << (2 * S)) - 1;
    u32 L = full | __builtin_bitreverse32(full);
    u32 F = L & ~(((lane ^ hh) & 1) ? 0u : 1u << ((lane & 31) >> 1));
    const u32 ob = (u32)(uintptr_t)(LAS char*)(o2 + row * ES + 8);
    SchedRound<32>::run(E, F, L, ob);
    STMP(1, 7);
    u32 m = m2[row];
    while (m) {
      const int rr = ffbl(m);
      m &= m - 1;
      const int i = 16 + ffbl(L >> 16);
      const u32 bit = 1u << i;
      L &= ~(bit | __builtin_bitreverse32(bit));
      *(LAS u16*)(uintptr_t)(ob + 2 * i) = *(const u16*)(e2 + row * ES + 8 + rr * 2);
    }
    STMP(1, 8);
    u32* img = (u32*)(ws + O_IMG) + (b * 24 + P) * 64 + lane;
#pragma unroll
    for (int s2 = 0; s2 < 3; s2++) {
      const u32x2 a = *(LAS const u32x2*)(uintptr_t)(ob + s2 * 8), c = *(LAS const u32x2*)(uintptr_t)(ob + 56 - s2 * 8);
#pragma unroll
      for (int k = 0; k < 2; k++) {
        const int s = s2 * 2 + k;
        const u32 hi = k ? c.x : c.y;
        const u32 w = (k ? a.y : a.x) | ((hi >> 16) | (hi << 16));
        const u32 z = (4 * s + hh) * 0x10001u + 0x20000u;
        const u32 o = max(w & 0xffffu, z & 0xffffu) | (max(w >> 16, z >> 16) << 16);
        if (s < S) img[s * 64] = o;
      }
    }
  } else if (wave == 3) {
    if (part == 0) {
      const u32 n0 = p2n[lane], n1 = p2n[64 + (lane & 15)];
      ((u32x2*)(ws + O_META))[b * 64 + lane] = (u32x2){n0 | ((u32)dis[b * 80 + n0] << 16), n1 | ((u32)dis[b * 80 + n1] << 16)};
    }
    if (lane == 0) ((u32*)(ws + O_OV2C))[b * 2 + part] = ovc[0];
  }
  STMP(1, 9);
  STMP(1, 10);
}

#define S_(x) #x
#define S(x) S_(x)
#define RS(p) "88+4*((" S(p) ")%%4)"
#define LD(p) \
  ".if " S(p) " < 24\n" \
  "v_lshlrev_b32_sdwa v104, 3, v[64+" S(p) "] dst_sel:DWORD dst_unused:UNUSED_PAD src0_sel:DWORD src1_sel:WORD_0\n" \
  "v_lshlrev_b32_sdwa v105, 3, v[64+" S(p) "] dst_sel:DWORD dst_unused:UNUSED_PAD src0_sel:DWORD src1_sel:WORD_1\n" \
  "ds_read_b64 v[" RS(p) ":" RS(p) "+1], v104\n" \
  "ds_read_b64 v[" RS(p) "+2:" RS(p) "+3], v105\n" \
  ".endif\n"
#define MF(p) \
  ".if (" S(p) ") == 6 || (" S(p) ") == 11 || (" S(p) ") == 16 || (" S(p) ") == 20\n" \
  "v_mov_b32_dpp v60, v60 row_ror:4 row_mask:0xf bank_mask:0xf\n" \
  "v_mov_b32_dpp v61, v61 row_ror:4 row_mask:0xf bank_mask:0xf\n" \
  "v_mov_b32_dpp v62, v62 row_ror:4 row_mask:0xf bank_mask:0xf\n" \
  "v_mov_b32_dpp v63, v63 row_ror:4 row_mask:0xf bank_mask:0xf\n" \
  "s_nop 1\n .endif\n" \
  ".if (" S(p) ") == 0\n v_mfma_f32_16x16x32_bf16 v[52:55], v[60:63], v[" RS(p) ":" RS(p) "+3], 0\n" \
  ".elseif (" S(p) ") < 20\n v_mfma_f32_16x16x32_bf16 v[52:55], v[60:63], v[" RS(p) ":" RS(p) "+3], v[52:55]\n" \
  ".elseif (" S(p) ") == 20\n v_mfma_f32_16x16x32_bf16 v[56:59], v[60:63], v[" RS(p) ":" RS(p) "+3], 0\n" \
  ".else\n v_mfma_f32_16x16x32_bf16 v[56:59], v[60:63], v[" RS(p) ":" RS(p) "+3], v[56:59]\n .endif\n"
#define PS(p) \
  ".if " S(p) " == 6 || " S(p) " == 12 || " S(p) " == 18\n s_setprio 3-" S(p) "/6\n .endif\n" \
  LD(p+3) \
  ".if " S(p) " < 22\n s_waitcnt lgkmcnt(4)\n .else\n s_waitcnt lgkmcnt(0)\n .endif\n" \
  MF(p) LD(p+4) MF(p+1)

__device__ __forceinline__ void gather(const u32x8* ix, u32x4& A, f32x4& a0, f32x4& a1) {
  asm volatile(
      "s_waitcnt lgkmcnt(0)\n"
      "s_setprio 3\n"
      LD(0) LD(1) LD(2)
      PS(0) PS(2) PS(4) PS(6) PS(8) PS(10) PS(12) PS(14) PS(16) PS(18) PS(20) PS(22)
      "s_setprio 0\n"
      "s_nop 7\n"
      : "={v[52:55]}"(a0), "={v[56:59]}"(a1), "+{v[60:63]}"(A)
      : "{v[64:71]}"(ix[0]), "{v[72:79]}"(ix[1]), "{v[80:87]}"(ix[2])
      : "memory", "v88", "v89", "v90", "v91", "v92", "v93", "v94", "v95", "v96", "v97", "v98", "v99", "v100", "v101", "v102", "v103",
        "v104", "v105");
}

constexpr int LDS_HOPS = 256 + TB + 16 * 1280;
__global__ __launch_bounds__(1024, 4) void k_hops(char* __restrict__ ws) {
  extern __shared__ __attribute__((aligned(16))) char lds[];
  const int tid = threadIdx.x, lane = tid & 63, wave = __builtin_amdgcn_readfirstlane(tid >> 6);
  const int b = blockIdx.x, xg = b & 7, j = b >> 3, slice = 4 * xg + (j & 3), range = j >> 2;
  const int bucket = range * 16 + wave;
  const bool active = bucket < NB;
  STMP(2, 0);
  const __amdgpu_buffer_rsrc_t rsG = __builtin_amdgcn_make_buffer_rsrc(ws + O_G, 0, 7 * 32 * TSTR, 0x00020000);
  const __amdgpu_buffer_rsrc_t rsH = __builtin_amdgcn_make_buffer_rsrc(ws + O_H, 0, 8 * 32 * TSTR, 0x00020000);
  const bool last = tid < (TB - 4 * 16384) / 16;
  const u32 lo = tid * 16, lo4 = last ? lo + 65536 : 0x80000000u;
  char* tab = lds + 256;
  u32x4 v[5];
#pragma unroll
  for (int i = 0; i < 5; i++) v[i] = __builtin_amdgcn_raw_buffer_load_b128(rsG, i < 4 ? lo + i * 16384 : lo4, slice * TSTR, 0);
  const int bk = active ? bucket : 0;
  u32x8 ix[3];
#pragma unroll
  for (int t = 0; t < 3; t++)
#pragma unroll
    for (int s = 0; s < 8; s++) ix[t][s] = ((const u32*)(ws + O_IMG))[(bk * 24 + t * 8 + s) * 64 + lane];
  const u32x2 mt = ((const u32x2*)(ws + O_META))[bk * 64 + lane];
  const u32x2 oc = ((const u32x2*)(ws + O_OV2C))[bk];
  const u32 c0 = __builtin_amdgcn_readfirstlane(oc.x), nov = c0 + __builtin_amdgcn_readfirstlane(oc.y);
  const u32* ov2 = (const u32*)(ws + O_OV2) + (size_t)bk * EE;
  u32 ove = 0;
  if (lane < nov) ove = ov2[lane < c0 ? lane : EE - 1 - (lane - c0)];
  const float dis0 = bfhi(mt.x), dis1 = bfhi(mt.y);
  const u32 n0 = mt.x & 0xffff, n1 = mt.y & 0xffff;
  u32x4 A;
  {
    const int r = lane & 15;
    const u32 one = (r >> 2) ? 0u : (r & 1) ? 0x3f800000u : 0x00003f80u;
    A.x = A.z = (r & 2) ? 0u : one;
    A.y = A.w = (r & 2) ? one : 0u;
  }
  char* stage = lds + 256 + TB + wave * 1280;
  if (tid < 16) *(u32x4*)(lds + tid * 16) = (u32x4){0, 0, 0, 0};
  u32x4 hprev = {0, 0, 0, 0};
  const u32 reg = slice * TSTR + bucket * 640 + lane * 16;
  const bool wr = active && lane < 40;
#pragma unroll 1
  for (int k = 1; k <= 7; k++) {
    {
      const u32 tb = ((k - 1) * 32 + slice) * TSTR;
      u32 pend = 0;
      STMP(2, k * 8);
#pragma unroll
      for (int i = 0; i < 5; i++) {
        const u32 m = max(max(v[i].x, v[i].y), max(v[i].z, v[i].w));
        if (i < 4 || last) *(u32x4*)(tab + lo + i * 16384) = v[i];
        if (__any(m == ~0u)) pend |= 1u << i;
      }
      if (k == 1) pend = 0;
      STMP(2, k * 8 + 1);
      u32 spins = 0;
      while (pend) {
        asm volatile("" ::: "memory");
        const int i = __builtin_ctz(pend);
        const u32x4 w = __builtin_amdgcn_raw_buffer_load_b128(rsG, i < 4 ? lo + i * 16384 : lo4, tb, 16);
        const u32 m = max(max(w.x, w.y), max(w.z, w.w));
        if (!__any(m == ~0u)) { pend &= pend - 1; if (i < 4 || last) *(u32x4*)(tab + lo + i * 16384) = w; }
        if (++spins > 4000000u) break;
      }
    }
    STMP(2, k * 8 + 2);
    __syncthreads();
    STMP(2, k * 8 + 3);
    if (wr) {
      if (k <= 5) { u32 m1 = ~0u; asm volatile("" : "+v"(m1)); __builtin_amdgcn_raw_buffer_store_b128((u32x4){m1, m1, m1, m1}, rsG, reg, (k + 1) * 32 * TSTR, 16); }
      if (k > 1) __builtin_amdgcn_raw_buffer_store_b128(hprev, rsH, reg, (k - 1) * 32 * TSTR, 0);
    }
    if (active) {
      if (nov) {
        *(u32x4*)(stage + lane * 16) = (u32x4){0, 0, 0, 0};
        if (lane < 16) *(u32x4*)(stage + 1024 + lane * 16) = (u32x4){0, 0, 0, 0};
        u32 e = ove;
        for (u32 base = 0;;) {
          if (base + lane < nov) {
            const u32x2 row = *(const u32x2*)(lds + (e & 0xffff) * 8);
            LAS float* p = (LAS float*)(stage + (e >> 16) * 16);
            __builtin_amdgcn_ds_faddf(p, bflo(row.x), 0, 0, false);
            __builtin_amdgcn_ds_faddf(p + 1, bfhi(row.x), 0, 0, false);
            __builtin_amdgcn_ds_faddf(p + 2, bflo(row.y), 0, 0, false);
            __builtin_amdgcn_ds_faddf(p + 3, bfhi(row.y), 0, 0, false);
          }
          base += 64;
          if (base >= nov) break;
          const u32 i = base + lane;
          e = i < nov ? ov2[i < c0 ? i : EE - 1 - (i - c0)] : 0u;
        }
      }
      f32x4 acc0, acc1;
      STMP(2, k * 8 + 4);
      gather(ix, A, acc0, acc1);
      STMP(2, k * 8 + 5);
      if (nov) {
        acc0 += *(const f32x4*)(stage + n0 * 16);
        acc1 += *(const f32x4*)(stage + n1 * 16);
        asm volatile("s_waitcnt lgkmcnt(0)" ::: "memory");
      }
      const u32x2 h0 = {pk_bf16(acc0[0] * dis0, acc0[1] * dis0), pk_bf16(acc0[2] * dis0, acc0[3] * dis0)};
      const u32x2 h1 = {pk_bf16(acc1[0] * dis1, acc1[1] * dis1), pk_bf16(acc1[2] * dis1, acc1[3] * dis1)};
      u32x2 g0 = {pk_bf16(bflo(h0.x) * dis0, bfhi(h0.x) * dis0), pk_bf16(bflo(h0.y) * dis0, bfhi(h0.y) * dis0)};
      u32x2 g1 = {pk_bf16(bflo(h1.x) * dis1, bfhi(h1.x) * dis1), pk_bf16(bflo(h1.y) * dis1, bfhi(h1.y) * dis1)};
      g0.x = min(g0.x, 0xfffffffeu); g0.y = min(g0.y, 0xfffffffeu); g1.x = min(g1.x, 0xfffffffeu); g1.y = min(g1.y, 0xfffffffeu);
      *(u32x2*)(stage + n0 * 8) = g0;
      *(u32x2*)(stage + 640 + n0 * 8) = h0;
      if (lane < 16) { *(u32x2*)(stage + n1 * 8) = g1; *(u32x2*)(stage + 640 + n1 * 8) = h1; }
      __builtin_amdgcn_wave_barrier();
      const int l40 = lane < 40 ? lane : 0;
      const u32x4 gv = *(const u32x4*)(stage + l40 * 16);
      hprev = *(const u32x4*)(stage + 640 + l40 * 16);
      asm volatile("s_waitcnt vmcnt(0)" ::: "memory");
      if (lane < 40) {
        if (k <= 6) __builtin_amdgcn_raw_buffer_store_b128(gv, rsG, reg, k * 32 * TSTR, 16);
        else __builtin_amdgcn_raw_buffer_store_b128(hprev, rsH, reg, 7 * 32 * TSTR, 0);
      }
    }
    STMP(2, k * 8 + 6);
    if (k == 7) break;
    asm volatile("s_waitcnt vmcnt(0)" ::: "memory");
    STMP(2, k * 8 + 7);
    __syncthreads();
    asm volatile("" ::: "memory");
#pragma unroll
    for (int i = 0; i < 5; i++) v[i] = __builtin_amdgcn_raw_buffer_load_b128(rsG, i < 4 ? lo + i * 16384 : lo4, (k * 32 + slice) * TSTR, 16);
  }
}

constexpr int HS = 10240 + 32;
constexpr int LDS_ATTN = 65536 + 8 * HS + 512;
__global__ __launch_bounds__(640) void k_attn(char* __restrict__ ws, const u16* __restrict__ wr, u16* __restrict__ out) {
  extern __shared__ __attribute__((aligned(16))) char lds[];
  const int tid = threadIdx.x, lane = tid & 63, wave = __builtin_amdgcn_readfirstlane(tid >> 6);
  const int nb = blockIdx.x * 40;
  STMP(3, 0);
  char* tk = lds + 65536;
  float* wc = (float*)(tk + 8 * HS);
  {
    const u32 sl = tid / 20, o = tid - sl * 20;
    const char* g = ws + O_H + (size_t)sl * TSTR + nb * 8 + o * 16;
#pragma unroll
    for (int k = 0; k < 8; k++)
      __builtin_amdgcn_global_load_lds((const GAS void*)(g + (size_t)k * 32 * TSTR), (LAS void*)(tk + k * HS + wave * 1024), 16, 0, 0);
#pragma unroll
    for (int i = 0; i < 7; i++) {
      const int c = i * 10 + wave;
      if (c < 64) __builtin_amdgcn_global_load_lds((const GAS void*)(ws + O_W + c * 1024 + lane * 16), (LAS void*)(lds + c * 1024), 16, 0, 0);
    }
  }
  if (wave == 0) {
    const u32 w2 = ((const u32*)wr)[lane];
    float s = bflo(w2) + bfhi(w2);
#pragma unroll
    for (int o = 32; o; o >>= 1) s += __shfl_xor(s, o);
    s *= 1.f / 128;
    const int f = lane * 2, p = ((f >> 2) & 1) * 64 + (f >> 3) * 4 + (f & 3);
    wc[p] = (bflo(w2) - s) * 1.4426950408889634f;
    wc[p + 1] = (bfhi(w2) - s) * 1.4426950408889634f;
  }
  STMP(3, 1);
  asm volatile("s_waitcnt vmcnt(0)" ::: "memory");
  STMP(3, 2);
  __syncthreads();
  STMP(3, 3);
  const int r = lane & 31, h = lane >> 5, m = r & 3, hop = r >> 2;
  u32x4 T[8];
  {
    const char* p = tk + hop * HS + h * 320 + (wave * 4 + m) * 8;
#pragma unroll
    for (int j = 0; j < 8; j++) {
      const u32x2 a = *(const u32x2*)(p + j * 4 * 320), c = *(const u32x2*)(p + (j * 4 + 2) * 320);
      T[j] = (u32x4){a.x, a.y, c.x, c.y};
    }
  }
  float rs;
  {
    float s = 0, q = 0;
    const bf16x2 ones = {(__bf16)1.f, (__bf16)1.f};
#pragma unroll
    for (int j = 0; j < 8; j++)
#pragma unroll
      for (int i = 0; i < 4; i++) {
        const u32 tw = T[j][i];
        const bf16x2 t = __builtin_bit_cast(bf16x2, tw);
        s = __builtin_amdgcn_fdot2_f32_bf16(t, ones, s, false);
        q = __builtin_amdgcn_fdot2_f32_bf16(t, t, q, false);
      }
    s += xor32f(s); q += xor32f(q);
    const float mu = s * (1.f / 128);
    rs = __builtin_amdgcn_rsqf(fmaxf(q * (1.f / 128) - mu * mu, 0.f) + 1e-5f);
  }
  STMP(3, 4);
  f32x16 sa = {};
#pragma unroll
  for (int bb = 0; bb < 4; bb++) {
    f32x16 u = {};
#pragma unroll
    for (int j = 0; j < 8; j++) u = mfma32(*(const u32x4*)(lds + ((bb * 8 + j) * 64 + lane) * 16), T[j], u);
    sa = mfma32(T[2 * bb], pack8(u, 0), sa);
    sa = mfma32(T[2 * bb + 1], pack8(u, 8), sa);
  }
  STMP(3, 5);
  u32x4 P0, P1;
  {
    float s[4], rl[4];
#pragma unroll
    for (int i = 0; i < 4; i++) {
      rl[i] = __uint_as_float(__builtin_amdgcn_ds_bpermute(4 * (m + 8 * i + 4 * h), __float_as_uint(rs)));
      const float lo = (m & 1) ? sa[4 * i + 1] : sa[4 * i], hi = (m & 1) ? sa[4 * i + 3] : sa[4 * i + 2];
      s[i] = ((m & 2) ? hi : lo) * rs * rl[i];
    }
    float mx = fmaxf(fmaxf(s[0], s[1]), fmaxf(s[2], s[3]));
    mx = fmaxf(mx, xor32f(mx));
    float sum = 0;
#pragma unroll
    for (int i = 0; i < 4; i++) { s[i] = __builtin_amdgcn_exp2f(s[i] - mx); sum += s[i]; }
    sum += xor32f(sum);
    const float inv = __builtin_amdgcn_rcpf(sum);
    const u32 mk = (m & 1) ? 0xffff0000u : 0xffffu, mA = (m & 2) ? 0u : mk, mB = (m & 2) ? mk : 0u;
    u32 pk[4];
#pragma unroll
    for (int i = 0; i < 4; i++) { const float p = s[i] * inv * rl[i]; pk[i] = pk_bf16(p, p); }
    P0 = (u32x4){pk[0] & mA, pk[0] & mB, pk[1] & mA, pk[1] & mB};
    P1 = (u32x4){pk[2] & mA, pk[2] & mB, pk[3] & mA, pk[3] & mB};
  }
  STMP(3, 6);
  f32x16 o[4];
  f32x2 s1 = {0, 0}, s2 = {0, 0}, sd = {0, 0};
#pragma unroll
  for (int bb = 0; bb < 4; bb++) {
    f32x16 vv = {};
#pragma unroll
    for (int j = 0; j < 8; j++) vv = mfma32(T[j], *(const u32x4*)(lds + 32768 + ((bb * 8 + j) * 64 + lane) * 16), vv);
    f32x16 c;
#pragma unroll
    for (int i = 0; i < 8; i++) { const u32 t = T[2 * bb + (i >> 2)][i & 3]; c[2 * i] = bflo(t); c[2 * i + 1] = bfhi(t); }
    c = mfma32(pack8(vv, 0), P0, c);
    c = mfma32(pack8(vv, 8), P1, c);
    o[bb] = c;
#pragma unroll
    for (int i = 0; i < 4; i++) {
      const f32x4 w = *(const f32x4*)(wc + h * 64 + bb * 16 + i * 4);
      const f32x2 x0 = {c[4 * i], c[4 * i + 1]}, x1 = {c[4 * i + 2], c[4 * i + 3]};
      s1 += x0; s1 += x1;
      s2 += x0 * x0; s2 += x1 * x1;
      sd += x0 * (f32x2){w.x, w.y}; sd += x1 * (f32x2){w.z, w.w};
    }
  }
  STMP(3, 7);
  float al;
  {
    float a = s1.x + s1.y, q = s2.x + s2.y, d = sd.x + sd.y;
    a += xor32f(a); q += xor32f(q); d += xor32f(d);
    const float mu = a * (1.f / 128);
    const float z = d * __builtin_amdgcn_rsqf(fmaxf(q * (1.f / 128) - mu * mu, 0.f) + 1e-5f);
    float mx = fmaxf(z, dppf<0x124>(z));
    mx = fmaxf(mx, dppf<0x128>(mx));
    { auto t = __builtin_amdgcn_permlane16_swap(__float_as_uint(mx), __float_as_uint(mx), false, false); mx = fmaxf(__uint_as_float(t[0]), __uint_as_float(t[1])); }
    const float e = __builtin_amdgcn_exp2f(z - mx);
    float sum = e + dppf<0x124>(e);
    sum += dppf<0x128>(sum);
    { auto t = __builtin_amdgcn_permlane16_swap(__float_as_uint(sum), __float_as_uint(sum), false, false); sum = __uint_as_float(t[0]) + __uint_as_float(t[1]); }
    al = e * __builtin_amdgcn_rcpf(sum);
  }
  float ae, ao;
  { auto t = __builtin_amdgcn_permlane16_swap(__float_as_uint(al), __float_as_uint(al), false, false); ae = __uint_as_float(t[0]); ao = __uint_as_float(t[1]); }
  float y[32];
#pragma unroll
  for (int g = 0; g < 2; g++)
#pragma unroll
    for (int i = 0; i < 16; i++) {
      auto t = __builtin_amdgcn_permlane16_swap(__float_as_uint(o[2 * g][i]), __float_as_uint(o[2 * g + 1][i]), false, false);
      y[g * 16 + i] = __uint_as_float(t[0]) * ae + __uint_as_float(t[1]) * ao;
    }
  float y2[16], y3[8];
#pragma unroll
  for (int g = 0; g < 2; g++)
#pragma unroll
    for (int i = 0; i < 8; i++) {
      const float a = y[g * 16 + i], c = y[g * 16 + 8 + i];
      const float a2 = a + dppf<0x128>(a), c2 = c + dppf<0x128>(c);
      y2[g * 8 + i] = (lane & 8) ? c2 : a2;
    }
#pragma unroll
  for (int g = 0; g < 2; g++)
#pragma unroll
    for (int i = 0; i < 4; i++) {
      const float a = y2[g * 8 + i], c = y2[g * 8 + 4 + i];
      const float a2 = a + dppf<0x104>(a), c2 = c + dppf<0x114>(c);
      y3[g * 4 + i] = (lane & 4) ? c2 : a2;
    }
  u16* op = out + (size_t)(nb + wave * 4 + m) * 128 + ((lane >> 4) & 1) * 32 + ((lane >> 3) & 1) * 16 + ((lane >> 2) & 1) * 8 + h * 4;
  *(u32x2*)op = (u32x2){pk_bf16(y3[0], y3[1]), pk_bf16(y3[2], y3[3])};
  *(u32x2*)(op + 64) = (u32x2){pk_bf16(y3[4], y3[5]), pk_bf16(y3[6], y3[7])};
  STMP(3, 8);
  STMP(3, 9);
}

extern "C" void kernel_launch(void* const* d_in, const int* in_sizes, int n_in, void* d_out, int out_size, void* d_ws, size_t ws_size,
                              hipStream_t stream) {
  char* ws = (char*)d_ws;
  (void)hipFuncSetAttribute((const void*)k_hops, hipFuncAttributeMaxDynamicSharedMemorySize, LDS_HOPS);
  (void)hipFuncSetAttribute((const void*)k_attn, hipFuncAttributeMaxDynamicSharedMemorySize, LDS_ATTN);
  k_prep<<<NW1 + NB, 1024, 0, stream>>>((const u16*)d_in[0], (const int*)d_in[1], (const int*)d_in[2], (const u16*)d_in[3], ws);
  k_sched<<<2 * NB + 2, 1024, 0, stream>>>((const u16*)d_in[3], (const u16*)d_in[4], (const u16*)d_in[5], (const u16*)d_in[6],
                                           (const u16*)d_in[7], ws);
  k_hops<<<256, 1024, LDS_HOPS, stream>>>(ws);
  k_attn<<<NN / 40, 640, LDS_ATTN, stream>>>(ws, (const u16*)d_in[8], (u16*)d_out);
}
```

```cpp
#include <hip/hip_runtime.h>
#include <stdint.h>

typedef unsigned short u16;
typedef unsigned u32;
typedef u32 u32x2 __attribute__((ext_vector_type(2)));
typedef u32 u32x4 __attribute__((ext_vector_type(4)));
typedef u32 u32x8 __attribute__((ext_vector_type(8)));
typedef float f32x2 __attribute__((ext_vector_type(2)));
typedef float f32x4 __attribute__((ext_vector_type(4)));
typedef float f32x16 __attribute__((ext_vector_type(16)));
typedef __bf16 bf16x2 __attribute__((ext_vector_type(2)));
typedef __bf16 bf16x8 __attribute__((ext_vector_type(8)));
#define LAS __attribute__((address_space(3)))
#define GAS __attribute__((address_space(1)))

constexpr int NN = 10000, EE = 320000, NB = 125, TSTR = 81920, TB = 80000;
constexpr int NW1 = 128, EPW = 2500, CAP1 = 48;
constexpr size_t O_G = 0;
constexpr size_t O_CELL = O_G + (size_t)8 * 32 * TSTR;
constexpr size_t O_OV1C = O_CELL + (size_t)NB * NW1 * CAP1 * 4;
constexpr size_t O_OV1 = O_OV1C + 512;
constexpr size_t O_IMG = O_OV1 + (size_t)EE * 4;
constexpr size_t O_META = O_IMG + (size_t)NB * 24 * 256;
constexpr size_t O_OV2C = O_META + (size_t)NB * 512;
constexpr size_t O_W = O_OV2C + 1024;
constexpr size_t O_OV2 = O_W + 65536 + 2048;

#define STMP(K, i)
__device__ __forceinline__ u32 pk_bf16(float a, float b) {
  f32x2 v = {a, b};
  return __builtin_bit_cast(u32, __builtin_convertvector(v, bf16x2));
}
__device__ __forceinline__ float bflo(u32 w) { return __uint_as_float(w << 16); }
__device__ __forceinline__ float bfhi(u32 w) { return __uint_as_float(w & 0xffff0000u); }
__device__ __forceinline__ f32x16 mfma32(u32x4 a, u32x4 b, f32x16 c) {
  return __builtin_amdgcn_mfma_f32_32x32x16_bf16(__builtin_bit_cast(bf16x8, a), __builtin_bit_cast(bf16x8, b), c, 0, 0, 0);
}
template <int CTRL> __device__ __forceinline__ u32 dpp(u32 v) { return __builtin_amdgcn_update_dpp(0u, v, CTRL, 0xf, 0xf, true); }
template <int CTRL> __device__ __forceinline__ float dppf(float v) { return __uint_as_float(dpp<CTRL>(__float_as_uint(v))); }
__device__ __forceinline__ float xor32f(float v) {
  auto r = __builtin_amdgcn_permlane32_swap(__float_as_uint(v), __float_as_uint(v), false, false);
  return __uint_as_float((threadIdx.x & 32) ? r[0] : r[1]);
}
__device__ __forceinline__ int ffbl(u32 v) { int r; asm("v_ffbl_b32 %0, %1" : "=v"(r) : "v"(v)); return r; }
__device__ __forceinline__ u32x4 pack8(const f32x16& a, int o) {
  return (u32x4){pk_bf16(a[o], a[o + 1]), pk_bf16(a[o + 2], a[o + 3]), pk_bf16(a[o + 4], a[o + 5]), pk_bf16(a[o + 6], a[o + 7])};
}

__global__ __launch_bounds__(1024) void k_prep(const u16* __restrict__ x, const int* __restrict__ src, const int* __restrict__ dst,
                                               const u16* __restrict__ dis, char* __restrict__ ws) {
  __shared__ __attribute__((aligned(16))) char lds[NB * CAP1 * 4 + 512];
  const int tid = threadIdx.x, b = blockIdx.x;
  STMP(0, 0);
  if (b < NW1) {
    u32* img = (u32*)lds;
    u32* cnt = img + NB * CAP1;
    u32x4 s4 = {0, 0, 0, 0}, d4 = {0, 0, 0, 0};
    if (tid < EPW / 4) { s4 = ((const u32x4*)src)[b * (EPW / 4) + tid]; d4 = ((const u32x4*)dst)[b * (EPW / 4) + tid]; }
    const u32x4 inv = {~0u, ~0u, ~0u, ~0u};
    ((u32x4*)img)[tid] = inv;
    if (tid < 1500 - 1024) ((u32x4*)img)[1024 + tid] = inv;
    if (tid < 128) cnt[tid] = 0;
    __syncthreads();
    STMP(0, 1);
    if (tid < EPW / 4) {
      u32 r[4], bk[4];
#pragma unroll
      for (int i = 0; i < 4; i++) { bk[i] = (d4[i] * 52429u) >> 22; r[i] = atomicAdd(&cnt[bk[i]], 1u); }
#pragma unroll
      for (int i = 0; i < 4; i++) {
        if (r[i] < CAP1) img[bk[i] * CAP1 + (r[i] & 7) * 6 + (r[i] >> 3)] = s4[i] | ((d4[i] - bk[i] * 80) << 16);
        else { u32 o = atomicAdd(&cnt[127], 1u); ((u32*)(ws + O_OV1))[b * EPW + o] = s4[i] | (d4[i] << 14); }
      }
    }
    STMP(0, 2);
    __syncthreads();
    STMP(0, 3);
#pragma unroll
    for (int it = 0; it < 2; it++) {
      const u32 i = it * 1024 + tid;
      if (i < 1500) { const u32 c = i / 12, p = i - c * 12; *(u32x4*)(ws + O_CELL + (size_t)(c * NW1 + b) * 192 + p * 16) = ((u32x4*)img)[i]; }
    }
    if (tid == 0) ((u32*)(ws + O_OV1C))[b] = cnt[127];
  } else {
    const int t = b - NW1;
    u32x4 v[2]; float dn[2];
#pragma unroll
    for (int it = 0; it < 2; it++) {
      const u32 i = it * 1024 + tid;
      if (i < 1280) { v[it] = ((const u32x4*)x)[t * 1280 + i]; dn[it] = __uint_as_float((u32)dis[t * 80 + (i >> 4)] << 16); }
    }
#pragma unroll
    for (int it = 0; it < 2; it++) {
      const u32 i = it * 1024 + tid;
      if (i < 1280) {
        const u32 sl = i / 40, o = i - sl * 40;
        *(u32x4*)(ws + O_G + (size_t)(32 + sl) * TSTR + t * 640 + o * 16) = (u32x4){~0u, ~0u, ~0u, ~0u};
        char* p = lds + (i & 15) * 1312 + (i >> 4) * 8;
        u32 g[4];
#pragma unroll
        for (int q = 0; q < 4; q++) g[q] = pk_bf16(bflo(v[it][q]) * dn[it], bfhi(v[it][q]) * dn[it]);
        *(u32x2*)p = (u32x2){g[0], g[1]};
        *(u32x2*)(p + 656) = (u32x2){g[2], g[3]};
      }
    }
    __syncthreads();
#pragma unroll
    for (int it = 0; it < 2; it++) {
      const u32 i = it * 1024 + tid;
      if (i < 1280) {
        const u32 sl = i / 40, o = i - sl * 40;
        *(u32x4*)(ws + O_G + (size_t)sl * TSTR + t * 640 + o * 16) = *(const u32x4*)(lds + sl * 656 + o * 16);
      }
    }
  }
  STMP(0, 4);
  STMP(0, 5);
}

__device__ __forceinline__ void fold_weights(const u16* __restrict__ wa, const u16* __restrict__ wb, const u16* __restrict__ wr, const bool isM,
                                             char* lds, char* __restrict__ ws) {
  const int tid = threadIdx.x, lane = tid & 63, w = tid >> 6, r = lane & 31, h = lane >> 5, db = w & 3, cb = w >> 2;
  constexpr int RS = 272, MB = 128 * RS;
  u32x4 t4[4];
#pragma unroll
  for (int i = 0; i < 4; i++) t4[i] = ((const u32x4*)(i < 2 ? wa : wb))[(i & 1) * 1024 + tid];
#pragma unroll
  for (int i = 0; i < 4; i++) *(u32x4*)(lds + (i >> 1) * MB + ((i & 1) * 64 + (tid >> 4)) * RS + (tid & 15) * 16) = t4[i];
  __syncthreads();
  f32x16 acc = {}, act = {};
  if (isM) {
#pragma unroll
    for (int j = 0; j < 8; j++) {
      const u32x4 a = *(const u32x4*)(lds + (db * 32 + r) * RS + j * 32 + h * 16);
      const u32x4 k = *(const u32x4*)(lds + MB + (cb * 32 + r) * RS + j * 32 + h * 16);
      acc = mfma32(a, k, acc);
      act = mfma32(k, a, act);
    }
  } else {
#pragma unroll
    for (int eb = 0; eb < 4; eb++) {
      f32x16 t = {};
#pragma unroll
      for (int jj = 0; jj < 2; jj++) {
        const u32x4 a = *(const u32x4*)(lds + MB + (eb * 32 + r) * RS + cb * 64 + jj * 32 + h * 16);
        const int k = r - jj * 16 - h * 8;
        u32x4 id;
#pragma unroll
        for (int q = 0; q < 4; q++) id[q] = k == 2 * q ? 0x3f80u : k == 2 * q + 1 ? 0x3f800000u : 0u;
        t = mfma32(a, id, t);
      }
#pragma unroll
      for (int jj = 0; jj < 2; jj++) {
        const char* p = lds + (db * 32 + r) * RS + eb * 64 + jj * 32 + h * 8;
        const u32x2 lo = *(const u32x2*)p, hi = *(const u32x2*)(p + 16);
        acc = mfma32((u32x4){lo.x, lo.y, hi.x, hi.y}, pack8(t, jj * 8), acc);
      }
    }
  }
  if (!isM) {
    float* y = (float*)(lds + 2 * MB);
    const u32 w2 = ((const u32*)wr)[lane];
    float sw = bflo(w2) + bfhi(w2);
#pragma unroll
    for (int o = 32; o; o >>= 1) sw += __shfl_xor(sw, o);
    const int e = tid >> 3, pt = tid & 7;
    float a = 0, c = 0;
#pragma unroll
    for (int q = 0; q < 8; q++) {
      const u32 m2 = *(const u32*)(lds + MB + e * RS + pt * 32 + q * 4), x2 = ((const u32*)wr)[pt * 8 + q];
      a += bflo(m2) + bfhi(m2);
      c += bflo(m2) * (bflo(x2) - sw * (1.f / 128)) + bfhi(m2) * (bfhi(x2) - sw * (1.f / 128));
    }
#pragma unroll
    for (int o = 4; o; o >>= 1) { a += __shfl_xor(a, o); c += __shfl_xor(c, o); }
    if (pt == 0) { y[e] = a; y[128 + e] = c; }
    __syncthreads();
    a = c = 0;
#pragma unroll
    for (int q = 0; q < 8; q++) {
      const u32 m2 = *(const u32*)(lds + e * RS + pt * 32 + q * 4);
      const int k = pt * 16 + q * 2;
      a += bflo(m2) * y[k] + bfhi(m2) * y[k + 1];
      c += bflo(m2) * y[128 + k] + bfhi(m2) * y[129 + k];
    }
#pragma unroll
    for (int o = 4; o; o >>= 1) { a += __shfl_xor(a, o); c += __shfl_xor(c, o); }
    if (pt == 0) { y[256 + e] = a; y[384 + e] = c; }
    __syncthreads();
    if (w == 0) {
      float ma = y[256 + lane] + y[320 + lane], mb = y[384 + lane] + y[448 + lane];
#pragma unroll
      for (int o = 32; o; o >>= 1) { ma += __shfl_xor(ma, o); mb += __shfl_xor(mb, o); }
      const int row = lane & 3, f0 = (lane >> 3) * 16 + ((lane >> 2) & 1) * 4;
      const float mean = (row == 2 ? ma : mb) * (1.f / 128);
      float v[8];
#pragma unroll
      for (int i = 0; i < 8; i++) {
        const int f = f0 + 8 * (i >> 2) + (i & 3);
        v[i] = row == 0 ? 1.f : row == 1 ? __uint_as_float((u32)wr[f] << 16) : y[row * 128 + f] - mean;
      }
      *(u32x4*)(ws + O_W + 65536 + lane * 16) = (u32x4){pk_bf16(v[0], v[1]), pk_bf16(v[2], v[3]), pk_bf16(v[4], v[5]), pk_bf16(v[6], v[7])};
      if (lane < 4) ((float*)(ws + O_W + 65536 + 1024))[lane] = sw * (1.f / 128);
    }
  }
  __syncthreads();
  float* pc = (float*)lds;
  float* pd = pc + 1024;
  float* md = pd + 1024;
  float s = 0, st = 0;
#pragma unroll
  for (int i = 0; i < 16; i++) { s += acc[i]; st += act[i]; }
  pc[(db * 2 + h) * 128 + cb * 32 + r] = s;
  pd[(cb * 2 + h) * 128 + db * 32 + r] = st;
  __syncthreads();
  if (tid < 128) { float a = 0;
#pragma unroll
    for (int i = 0; i < 8; i++) a += pd[i * 128 + tid];
    md[tid] = a * (1.f / 128); }
  float mc = 0;
#pragma unroll
  for (int i = 0; i < 8; i++) mc += pc[i * 128 + cb * 32 + r];
  mc *= 1.f / 128;
  __syncthreads();
  float tm = md[lane] + md[lane + 64];
#pragma unroll
  for (int o = 32; o; o >>= 1) tm += __shfl_xor(tm, o);
  tm *= 1.f / 128;
  const float sc = isM ? 0.08838834764831845f * 1.4426950408889634f : 1.f;
#pragma unroll
  for (int i = 0; i < 16; i++) acc[i] = (acc[i] - mc - md[db * 32 + (i & 3) + 8 * (i >> 2) + 4 * h] + tm) * sc;
  char* o = ws + O_W + (isM ? 0 : 32768) + ((cb * 8 + db * 2) * 64 + lane) * 16;
  *(u32x4*)o = pack8(acc, 0);
  *(u32x4*)(o + 1024) = pack8(acc, 8);
}

template <int R> struct SchedRound {
  static __device__ __forceinline__ void run(const u32* E, u32& F, u32& L, u32 obase) {
    SchedRound<R - 1>::run(E, F, L, obase);
    constexpr int r = R - 1;
    if (r > 0) {
      constexpr int z = __builtin_ctz(r | 32);
      if (z == 0) F = dpp<0xB1>(F);
      else if (z == 1) F = dpp<0x4E>(F);
      else if (z == 2) F = dpp<0x141>(F);
      else if (z == 3) F = dpp<0x128>(F);
      else F = __builtin_amdgcn_ds_swizzle(F, 0x401F);
    }
    const u32 w = E[r >> 1];
    const u32 e = (r & 1) ? w >> 16 : w & 0xffffu;
    const u32 has = min(e, 1u);
    const int i = max(ffbl(F & L), ffbl(L));
    const u32 bit = has << (i & 31);
    F &= ~bit;
    L &= ~(bit | __builtin_bitreverse32(bit));
    *(LAS u16*)(uintptr_t)(obase + 2 * i) = (u16)e;
  }
};
template <> struct SchedRound<0> { static __device__ __forceinline__ void run(const u32*, u32&, u32&, u32) {} };

__global__ __launch_bounds__(1024) void k_sched(const u16* __restrict__ dis, const u16* __restrict__ wq, const u16* __restrict__ wk,
                                                const u16* __restrict__ wv, const u16* __restrict__ wo, const u16* __restrict__ wr,
                                                char* __restrict__ ws) {
  constexpr int ES = 72;
  constexpr int ZB = 10240 + 2 * 192 * ES + 768 + 16;
  __shared__ __attribute__((aligned(16))) char lds[2 * 128 * 272 + 2048];
  if (blockIdx.x >= 2 * NB) {
    STMP(1, 0);
    if (blockIdx.x == 2 * NB) fold_weights(wq, wk, wr, true, lds, ws); else fold_weights(wv, wo, wr, false, lds, ws);
    STMP(1, 11);
    return;
  }
  char* zer = lds;
  char* e2 = lds + ZB;
  u16* info = (u16*)(e2 + 192 * ES);
  u16* key = info + 2560;
  unsigned char* p2n = (unsigned char*)(key + 80);
  u32* cnt = (u32*)zer;
  char* e1 = zer + 10240;
  char* o2 = e1 + 192 * ES;
  u32* m2 = (u32*)(o2 + 192 * ES);
  u32* ovc = m2 + 192;
  const int tid = threadIdx.x, lane = tid & 63, wave = __builtin_amdgcn_readfirstlane(tid >> 6);
  const int b = blockIdx.x >> 1, part = blockIdx.x & 1;
  STMP(1, 0);
  u32 en[6];
  {
    const u32x2* p = (const u32x2*)(ws + O_CELL + (size_t)b * NW1 * 192 + tid * 24);
    const u32x2 a0 = p[0], a1 = p[1], a2 = p[2];
    en[0] = a0.x; en[1] = a0.y; en[2] = a1.x; en[3] = a1.y; en[4] = a2.x; en[5] = a2.y;
  }
  u32 c1 = 0;
  if (tid < NW1) c1 = ((const u32*)(ws + O_OV1C))[tid];
  constexpr int NZ = ZB / 16;
#pragma unroll
  for (int it = 0; it < (NZ + 1023) / 1024; it++) if (it * 1024 + tid < NZ) ((u32x4*)zer)[it * 1024 + tid] = (u32x4){0, 0, 0, 0};
  __syncthreads();
  STMP(1, 1);
  if (c1) ovc[1] = 1;
#pragma unroll
  for (int i = 0; i < 6; i++) {
    if (en[i] != ~0u) {
      const u32 cell = (en[i] >> 16) * 32 + (en[i] & 31);
      const u32 r = atomicAdd(&cnt[cell], 1u);
      en[i] = (en[i] & 0x3fff) | (cell << 14) | (min(r, 8u) << 26);
    }
  }
  STMP(1, 2);
  __syncthreads();
  STMP(1, 3);
  u32 pre[4];
  const int node = tid >> 3, q8 = tid & 7;
  if (tid < 640) {
    const u32x4 c = ((const u32x4*)cnt)[tid];
    const u32 c0 = min(c.x, 8u), cc1 = min(c.y, 8u), c2 = min(c.z, 8u), c3 = min(c.w, 8u);
    const u32 tot = c0 + cc1 + c2 + c3;
    u32 s = tot, t;
    t = dpp<0x111>(s); s += q8 >= 1 ? t : 0;
    t = dpp<0x112>(s); s += q8 >= 2 ? t : 0;
    t = dpp<0x114>(s); s += q8 >= 4 ? t : 0;
    pre[0] = s - tot; pre[1] = pre[0] + c0; pre[2] = pre[1] + cc1; pre[3] = pre[2] + c2;
    if (q8 == 7) key[node] = (u16)((s << 7) | (127 - node));
  }
  __syncthreads();
  if (tid < 640) {
    const u32 mine = key[node];
    const u32* kp = (const u32*)key + q8 * 5;
    u32 n = 0;
#pragma unroll
    for (int i = 0; i < 5; i++) { const u32 k2 = kp[i]; n += ((k2 & 0xffff) > mine) + ((k2 >> 16) > mine); }
    n += dpp<0xB1>(n); n += dpp<0x4E>(n); n += dpp<0x141>(n);
    const u32 ps = n << 8;
    ((u32x2*)info)[tid] = (u32x2){(pre[0] | ps) | ((pre[1] | ps) << 16), (pre[2] | ps) | ((pre[3] | ps) << 16)};
    if (q8 == 0) p2n[n] = (unsigned char)node;
  }
  __syncthreads();
  STMP(1, 4);
  u32* ov2 = (u32*)(ws + O_OV2) + (size_t)b * EE;
#pragma unroll
  for (int i = 0; i < 6; i++) {
    if (en[i] != ~0u) {
      const u32 cell = (en[i] >> 14) & 0xfff, r = en[i] >> 26, val = (en[i] & 0x3fff) + 32;
      const u32 inf = info[cell], pos = inf >> 8, tile = pos >> 4, col = pos & 15, p = (inf & 255) + r;
      const u32 tl = tile - 3 * part;
      if (tl < 3u - part) {
        if (r >= 8 || p >= 48 - 8 * ((tile + 1) >> 1)) {
          const u32 o = atomicAdd(&ovc[0], 1u);
          ov2[part ? EE - 1 - o : o] = val | ((cell >> 5) << 16);
        } else {
          const u32 q = (p + col) & 3, ln = col + ((q & 1) << 5) + ((q & 2) << 3);
          const u32 xx = (cell ^ ln) & 31, g = xx ^ (((xx >> 2) & 1) * 3);
          u32 rr = g ^ (g >> 1); rr ^= rr >> 2; rr ^= rr >> 4;
          const u32 row = tl * 64 + ln;
          if (r < 4) *(u16*)(e1 + row * ES + 8 + rr * 2) = (u16)val;
          else { *(u16*)(e2 + row * ES + 8 + rr * 2) = (u16)val; atomicOr(&m2[row], 1u << rr); }
        }
      }
    }
  }
  if (part == 0 && ovc[1]) {
    for (int w = 0; w < NW1; w++) {
      const u32 n = ((const u32*)(ws + O_OV1C))[w];
      for (u32 i = tid; i < n; i += 1024) {
        const u32 e = ((const u32*)(ws + O_OV1))[w * EPW + i], d = e >> 14;
        if (d - b * 80 < 80u) { const u32 o = atomicAdd(&ovc[0], 1u); ov2[o] = ((e & 0x3fff) + 32) | ((d - b * 80) << 16); }
      }
    }
  }
  STMP(1, 5);
  __syncthreads();
  STMP(1, 6);
  if (wave < 3 - part) {
    const int tile = wave + 3 * part, S = 6 - ((tile + 1) >> 1), P = tile == 0 ? 0 : tile == 4 ? 20 : 5 * tile + 1;
    const u32 row = wave * 64 + lane, hh = lane >> 5;
    u32 E[16];
#pragma unroll
    for (int i = 0; i < 8; i++) { const u32x2 t = *(const u32x2*)(e1 + row * ES + 8 + i * 8); E[2 * i] = t.x; E[2 * i + 1] = t.y; }
    const u32 full = (1u << (2 * S)) - 1;
    u32 L = full | __builtin_bitreverse32(full);
    u32 F = L & ~(((lane ^ hh) & 1) ? 0u : 1u << ((lane & 31) >> 1));
    const u32 ob = (u32)(uintptr_t)(LAS char*)(o2 + row * ES + 8);
    SchedRound<32>::run(E, F, L, ob);
    STMP(1, 7);
    u32 m = m2[row];
    while (m) {
      const int rr = ffbl(m);
      m &= m - 1;
      const int i = 16 + ffbl(L >> 16);
      const u32 bit = 1u << i;
      L &= ~(bit | __builtin_bitreverse32(bit));
      *(LAS u16*)(uintptr_t)(ob + 2 * i) = *(const u16*)(e2 + row * ES + 8 + rr * 2);
    }
    STMP(1, 8);
    u32* img = (u32*)(ws + O_IMG) + (b * 24 + P) * 64 + lane;
#pragma unroll
    for (int s2 = 0; s2 < 3; s2++) {
      const u32x2 a = *(LAS const u32x2*)(uintptr_t)(ob + s2 * 8), c = *(LAS const u32x2*)(uintptr_t)(ob + 56 - s2 * 8);
#pragma unroll
      for (int k = 0; k < 2; k++) {
        const int s = s2 * 2 + k;
        const u32 hi = k ? c.x : c.y;
        const u32 w = (k ? a.y : a.x) | ((hi >> 16) | (hi << 16));
        const u32 z = (4 * s + hh) * 0x10001u + 0x20000u;
        const u32 o = max(w & 0xffffu, z & 0xffffu) | (max(w >> 16, z >> 16) << 16);
        if (s < S) img[s * 64] = o;
      }
    }
  } else if (wave == 3) {
    if (part == 0) {
      const u32 n0 = p2n[lane], n1 = p2n[64 + (lane & 15)];
      ((u32x2*)(ws + O_META))[b * 64 + lane] = (u32x2){n0 | ((u32)dis[b * 80 + n0] << 16), n1 | ((u32)dis[b * 80 + n1] << 16)};
    }
    if (lane == 0) ((u32*)(ws + O_OV2C))[b * 2 + part] = ovc[0];
  }
  STMP(1, 9);
  STMP(1, 10);
}

#define S_(x) #x
#define S(x) S_(x)
#define RS(p) "88+4*((" S(p) ")%%4)"
#define LD(p) \
  ".if " S(p) " < 24\n" \
  "v_lshlrev_b32_sdwa v104, 3, v[64+" S(p) "] dst_sel:DWORD dst_unused:UNUSED_PAD src0_sel:DWORD src1_sel:WORD_0\n" \
  "v_lshlrev_b32_sdwa v105, 3, v[64+" S(p) "] dst_sel:DWORD dst_unused:UNUSED_PAD src0_sel:DWORD src1_sel:WORD_1\n" \
  "ds_read_b64 v[" RS(p) ":" RS(p) "+1], v104\n" \
  "ds_read_b64 v[" RS(p) "+2:" RS(p) "+3], v105\n" \
  ".endif\n"
#define MF(p) \
  ".if (" S(p) ") == 6 || (" S(p) ") == 11 || (" S(p) ") == 16 || (" S(p) ") == 20\n" \
  "v_mov_b32_dpp v60, v60 row_ror:4 row_mask:0xf bank_mask:0xf\n" \
  "v_mov_b32_dpp v61, v61 row_ror:4 row_mask:0xf bank_mask:0xf\n" \
  "v_mov_b32_dpp v62, v62 row_ror:4 row_mask:0xf bank_mask:0xf\n" \
  "v_mov_b32_dpp v63, v63 row_ror:4 row_mask:0xf bank_mask:0xf\n" \
  "s_nop 1\n .endif\n" \
  ".if (" S(p) ") == 0\n v_mfma_f32_16x16x32_bf16 v[52:55], v[60:63], v[" RS(p) ":" RS(p) "+3], 0\n" \
  ".elseif (" S(p) ") < 20\n v_mfma_f32_16x16x32_bf16 v[52:55], v[60:63], v[" RS(p) ":" RS(p) "+3], v[52:55]\n" \
  ".elseif (" S(p) ") == 20\n v_mfma_f32_16x16x32_bf16 v[56:59], v[60:63], v[" RS(p) ":" RS(p) "+3], 0\n" \
  ".else\n v_mfma_f32_16x16x32_bf16 v[56:59], v[60:63], v[" RS(p) ":" RS(p) "+3], v[56:59]\n .endif\n"
#define PS(p) \
  ".if " S(p) " == 6 || " S(p) " == 12 || " S(p) " == 18\n s_setprio 3-" S(p) "/6\n .endif\n" \
  LD(p+3) \
  ".if " S(p) " < 22\n s_waitcnt lgkmcnt(4)\n .else\n s_waitcnt lgkmcnt(0)\n .endif\n" \
  MF(p) LD(p+4) MF(p+1)

__device__ __forceinline__ void gather(const u32x8* ix, u32x4& A, f32x4& a0, f32x4& a1) {
  asm volatile(
      "s_waitcnt lgkmcnt(0)\n"
      "s_setprio 3\n"
      LD(0) LD(1) LD(2)
      PS(0) PS(2) PS(4) PS(6) PS(8) PS(10) PS(12) PS(14) PS(16) PS(18) PS(20) PS(22)
      "s_setprio 0\n"
      "s_nop 7\n"
      : "={v[52:55]}"(a0), "={v[56:59]}"(a1), "+{v[60:63]}"(A)
      : "{v[64:71]}"(ix[0]), "{v[72:79]}"(ix[1]), "{v[80:87]}"(ix[2])
      : "memory", "v88", "v89", "v90", "v91", "v92", "v93", "v94", "v95", "v96", "v97", "v98", "v99", "v100", "v101", "v102", "v103",
        "v104", "v105");
}

constexpr int L_TAB = 256, L_STG = L_TAB + TB, L_TOK = L_STG + 16 * 1280, HS = 10240 + 32, L_FUN = L_TOK + 6 * HS;
constexpr int L_TK6 = 65536 + 64, L_SCR = 90112, LDS_MAIN = L_FUN + 1024 + 16;
static_assert((L_TK6 - L_TOK - 6 * HS) % 256 == 0 && L_TK6 + HS <= L_STG && L_TK6 + 2 * HS <= L_SCR && LDS_MAIN <= 163840, "lds map");

__device__ __forceinline__ bool marked(u32x4 v) { return __any(max(max(v.x, v.y), max(v.z, v.w)) == ~0u); }
__device__ __forceinline__ float sum32(float v) {
  auto r = __builtin_amdgcn_permlane32_swap(__float_as_uint(v), __float_as_uint(v), false, false);
  return __uint_as_float(r[0]) + __uint_as_float(r[1]);
}
__device__ __forceinline__ float max32(float v) {
  auto r = __builtin_amdgcn_permlane32_swap(__float_as_uint(v), __float_as_uint(v), false, false);
  return fmaxf(__uint_as_float(r[0]), __uint_as_float(r[1]));
}
__device__ __forceinline__ float bsel(u32 m, float a, float b) { return __uint_as_float((__float_as_uint(a) & ~m) | (__float_as_uint(b) & m)); }

__device__ __forceinline__ void attn_tile(char* lds, const int tile, const float ds, const float sc, u16* __restrict__ op) {
  const int lane = threadIdx.x & 63, r = lane & 31, h = lane >> 5, m = r & 3, hop = r >> 2;
  u32x4 T[8];
  {
    const char* p = lds + (hop < 6 ? L_TOK : L_TK6 - 6 * HS) + hop * HS + h * 320 + (tile * 4 + m) * 8;
#pragma unroll
    for (int j = 0; j < 8; j++) {
      const u32x2 a = *(const u32x2*)(p + j * 4 * 320), c = *(const u32x2*)(p + (j * 4 + 2) * 320);
      T[j] = (u32x4){a.x, a.y, c.x, c.y};
    }
  }
  const float wbar = *(const float*)(lds + L_FUN + 1024);
  char* scr = lds + L_SCR + tile * 512;
  const float eps = 1e-5f * ds * ds;
  float sy, yw, rs;
  {
    f32x16 f = {};
#pragma unroll
    for (int j = 0; j < 8; j++) f = mfma32(*(const u32x4*)(lds + L_FUN + j * 128 + h * 64 + m * 16), T[j], f);
    float q = 0, q2 = 0;
#pragma unroll
    for (int j = 0; j < 8; j++)
#pragma unroll
      for (int i = 0; i < 4; i += 2) {
        const u32 t0 = T[j][i], t1 = T[j][i + 1];
        q = __builtin_amdgcn_fdot2_f32_bf16(__builtin_bit_cast(bf16x2, t0), __builtin_bit_cast(bf16x2, t0), q, false);
        q2 = __builtin_amdgcn_fdot2_f32_bf16(__builtin_bit_cast(bf16x2, t1), __builtin_bit_cast(bf16x2, t1), q2, false);
      }
    q = sum32(q + q2);
    sy = f[0]; yw = f[1];
    const float mu = sy * (1.f / 128);
    rs = __builtin_copysignf(__builtin_amdgcn_rsqf(fmaxf(q * (1.f / 128) - mu * mu, 0.f) + eps), ds);
    if (h == 0) *(f32x4*)(scr + r * 16) = (f32x4){rs, f[2], f[3], 0.f};
  }
  float s[4];
  {
    f32x16 sa = {};
#pragma unroll
    for (int bb = 0; bb < 4; bb++) {
      f32x16 u = {};
#pragma unroll
      for (int j = 0; j < 8; j++) u = mfma32(*(const u32x4*)(lds + ((bb * 8 + j) * 64 + lane) * 16), T[j], u);
      sa = mfma32(T[2 * bb], pack8(u, 0), sa);
      sa = mfma32(T[2 * bb + 1], pack8(u, 8), sa);
    }
    u32 m1 = (m & 1) ? ~0u : 0u, m2 = (m & 2) ? ~0u : 0u;
    asm("" : "+v"(m1), "+v"(m2));
#pragma unroll
    for (int i = 0; i < 4; i++) s[i] = bsel(m2, bsel(m1, sa[4 * i], sa[4 * i + 1]), bsel(m1, sa[4 * i + 2], sa[4 * i + 3]));
  }
  u32x4 V[8];
#pragma unroll
  for (int bb = 0; bb < 4; bb++) {
    f32x16 vv = {};
#pragma unroll
    for (int j = 0; j < 8; j++) vv = mfma32(T[j], *(const u32x4*)(lds + 32768 + ((bb * 8 + j) * 64 + lane) * 16), vv);
    V[2 * bb] = pack8(vv, 0); V[2 * bb + 1] = pack8(vv, 8);
  }
  u32x4 P0, P1;
  float s1, sd;
  {
    f32x4 ky[4];
#pragma unroll
    for (int i = 0; i < 4; i++) { ky[i] = *(const f32x4*)(scr + (m + 8 * i + 4 * h) * 16); s[i] = s[i] * rs * ky[i].x; }
    const float mx = max32(fmaxf(fmaxf(s[0], s[1]), fmaxf(s[2], s[3])));
#pragma unroll
    for (int i = 0; i < 4; i++) s[i] = __builtin_amdgcn_exp2f(s[i] - mx);
    const float inv = ds * __builtin_amdgcn_rcpf(sum32((s[0] + s[1]) + (s[2] + s[3])));
    u32 mA = (m & 2) ? 0u : (m & 1) ? 0xffff0000u : 0xffffu, mB = (m & 2) ? ((m & 1) ? 0xffff0000u : 0xffffu) : 0u;
    u32 pk[4];
    float a = 0, c = 0;
#pragma unroll
    for (int i = 0; i < 4; i++) { const float p = s[i] * inv * ky[i].x; pk[i] = pk_bf16(p, p); a += p * ky[i].y; c += p * ky[i].z; }
    P0 = (u32x4){pk[0] & mA, pk[0] & mB, pk[1] & mA, pk[1] & mB};
    P1 = (u32x4){pk[2] & mA, pk[2] & mB, pk[3] & mA, pk[3] & mB};
    s1 = sy + sum32(a);
    sd = yw - wbar * sy + sum32(c);
  }
  f32x16 o[4];
  float q0 = 0, q1 = 0;
#pragma unroll
  for (int bb = 0; bb < 4; bb++) {
    f32x16 c;
#pragma unroll
    for (int i = 0; i < 8; i++) { const u32 t = T[2 * bb + (i >> 2)][i & 3]; c[2 * i] = bflo(t); c[2 * i + 1] = bfhi(t); }
    c = mfma32(V[2 * bb], P0, c);
    c = mfma32(V[2 * bb + 1], P1, c);
    o[bb] = c;
#pragma unroll
    for (int i = 0; i < 16; i += 2) {
      q0 = __builtin_fmaf(c[i], c[i], q0); asm("" : "+v"(q0));
      q1 = __builtin_fmaf(c[i + 1], c[i + 1], q1);
    }
  }
  float al;
  {
    const float mu = s1 * (1.f / 128);
    const float z = sd * __builtin_copysignf(1.4426950408889634f, ds) * __builtin_amdgcn_rsqf(fmaxf(sum32(q0 + q1) * (1.f / 128) - mu * mu, 0.f) + eps);
    float mx = fmaxf(z, dppf<0x124>(z));
    mx = fmaxf(mx, dppf<0x128>(mx));
    { auto t = __builtin_amdgcn_permlane16_swap(__float_as_uint(mx), __float_as_uint(mx), false, false); mx = fmaxf(__uint_as_float(t[0]), __uint_as_float(t[1])); }
    const float e = __builtin_amdgcn_exp2f(z - mx);
    float sum = e + dppf<0x124>(e);
    sum += dppf<0x128>(sum);
    { auto t = __builtin_amdgcn_permlane16_swap(__float_as_uint(sum), __float_as_uint(sum), false, false); sum = __uint_as_float(t[0]) + __uint_as_float(t[1]); }
    al = e * sc * __builtin_amdgcn_rcpf(sum);
  }
  float ae, ao;
  { auto t = __builtin_amdgcn_permlane16_swap(__float_as_uint(al), __float_as_uint(al), false, false); ae = __uint_as_float(t[0]); ao = __uint_as_float(t[1]); }
#pragma unroll
  for (int g = 0; g < 2; g++) {
    float y[16];
#pragma unroll
    for (int i = 0; i < 16; i++) {
      auto t = __builtin_amdgcn_permlane16_swap(__float_as_uint(o[2 * g][i]), __float_as_uint(o[2 * g + 1][i]), false, false);
      float a = __uint_as_float(t[0]) * ae;
      asm("" : "+v"(a));
      y[i] = __builtin_fmaf(__uint_as_float(t[1]), ao, a);
    }
#define A8(i, j) "v_add_f32_dpp %" #i ", %" #i ", %" #i " row_ror:8 row_mask:0xf bank_mask:0x3\n v_add_f32_dpp %" #i ", %" #j ", %" #j " row_ror:8 row_mask:0xf bank_mask:0xc\n"
#define A4(i, j) "v_add_f32_dpp %" #i ", %" #i ", %" #i " row_shl:4 row_mask:0xf bank_mask:0x5\n v_add_f32_dpp %" #i ", %" #j ", %" #j " row_shr:4 row_mask:0xf bank_mask:0xa\n"
    asm("s_nop 1\n" A8(0, 8) A8(1, 9) A8(2, 10) A8(3, 11) A8(4, 12) A8(5, 13) A8(6, 14) A8(7, 15) "s_nop 1\n" A4(0, 4) A4(1, 5) A4(2, 6) A4(3, 7)
        : "+v"(y[0]), "+v"(y[1]), "+v"(y[2]), "+v"(y[3]), "+v"(y[4]), "+v"(y[5]), "+v"(y[6]), "+v"(y[7])
        : "v"(y[8]), "v"(y[9]), "v"(y[10]), "v"(y[11]), "v"(y[12]), "v"(y[13]), "v"(y[14]), "v"(y[15]));
    *(u32x2*)(op + g * 64) = (u32x2){pk_bf16(y[0], y[1]), pk_bf16(y[2], y[3])};
  }
}

__global__ __launch_bounds__(1024, 4) void k_main(const u16* __restrict__ x, const u16* __restrict__ dis, char* ws, u16* __restrict__ out) {
  extern __shared__ __attribute__((aligned(16))) char lds[];
  const int tid = threadIdx.x, lane = tid & 63, wave = __builtin_amdgcn_readfirstlane(tid >> 6);
  const int b = blockIdx.x, xg = b & 7, j = b >> 3, slice = 4 * xg + (j & 3), range = j >> 2;
  const int bucket = range * 16 + wave;
  const bool active = bucket < NB;
  STMP(2, 0);
  const __amdgpu_buffer_rsrc_t rsG = __builtin_amdgcn_make_buffer_rsrc(ws + O_G, 0, 8 * 32 * TSTR, 0x00020000);
  const bool last = tid < (TB - 4 * 16384) / 16;
  const u32 lo = tid * 16, lo4 = last ? lo + 65536 : 0x80000000u;
  char* tab = lds + L_TAB;
  u32x4 v[5];
#pragma unroll
  for (int i = 0; i < 5; i++) v[i] = __builtin_amdgcn_raw_buffer_load_b128(rsG, i < 4 ? lo + i * 16384 : lo4, slice * TSTR, 0);
  const int bk = active ? bucket : 0;
  u32x8 ix[3];
#pragma unroll
  for (int t = 0; t < 3; t++)
#pragma unroll
    for (int s = 0; s < 8; s++) ix[t][s] = ((const u32*)(ws + O_IMG))[(bk * 24 + t * 8 + s) * 64 + lane];
  const u32x2 mt = ((const u32x2*)(ws + O_META))[bk * 64 + lane];
  const u32x2 oc = ((const u32x2*)(ws + O_OV2C))[bk];
  const bool tk = wave < 10 && b < NN / 40;
  const u32 tko = (tid / 20) * TSTR + b * 320 + (tid % 20) * 16;
  if (tk) {
    const u32x4 xv = ((const u32x4*)x)[b * 640 + tid];
    char* p = lds + L_TOK + (tid & 15) * 640 + (tid >> 4) * 8;
    *(u32x2*)p = (u32x2){xv.x, xv.y};
    *(u32x2*)(p + 320) = (u32x2){xv.z, xv.w};
  } else if (wave == 10) {
    *(u32x4*)(lds + L_FUN + lane * 16) = *(const u32x4*)(ws + O_W + 65536 + lane * 16);
    if (lane == 0) *(u32x4*)(lds + L_FUN + 1024) = *(const u32x4*)(ws + O_W + 65536 + 1024);
  }
  const u32 c0 = __builtin_amdgcn_readfirstlane(oc.x), nov = c0 + __builtin_amdgcn_readfirstlane(oc.y);
  const u32* ov2 = (const u32*)(ws + O_OV2) + (size_t)bk * EE;
  const float dis0 = bfhi(mt.x) * bfhi(mt.x), dis1 = bfhi(mt.y) * bfhi(mt.y);
  const u32 n0 = mt.x & 0xffff, n1 = mt.y & 0xffff;
  u32x4 A;
  {
    const int r = lane & 15;
    const u32 one = (r >> 2) ? 0u : (r & 1) ? 0x3f800000u : 0x00003f80u;
    A.x = A.z = (r & 2) ? 0u : one;
    A.y = A.w = (r & 2) ? one : 0u;
  }
  char* stage = lds + L_STG + wave * 1280;
  if (tid < 16) *(u32x4*)(lds + tid * 16) = (u32x4){0, 0, 0, 0};
  const u32 reg = slice * TSTR + bucket * 640 + lane * 16;
  const bool wr = active && lane < 40;
  auto token = [&](u32x4 t, int k, int dst) {
    u32 spins = 0;
    while (marked(t) && ++spins < 4000000u) {
      u32 o = tko;
      asm volatile("" : "+v"(o) :: "memory");
      t = __builtin_amdgcn_raw_buffer_load_b128(rsG, o, k * 32 * TSTR, 16);
    }
    *(u32x4*)(lds + dst + tid * 16) = t;
  };
#pragma unroll 1
  for (int k = 1;; k++) {
    {
      const u32 tb = ((k - 1) * 32 + slice) * TSTR;
      u32 pend = 0;
      STMP(2, k * 8);
#pragma unroll
      for (int i = 0; i < 5; i++) {
        if (i < 4 || last) *(u32x4*)(tab + lo + i * 16384) = v[i];
        if (marked(v[i])) pend |= 1u << i;
      }
      if (k == 1) pend = 0;
      STMP(2, k * 8 + 1);
      u32 spins = 0;
      while (pend) {
        const int i = __builtin_ctz(pend);
        u32 o = i < 4 ? lo + i * 16384 : lo4;
        asm volatile("" : "+v"(o) :: "memory");
        const u32x4 w = __builtin_amdgcn_raw_buffer_load_b128(rsG, o, tb, 16);
        if (!marked(w)) { pend &= pend - 1; if (i < 4 || last) *(u32x4*)(tab + lo + i * 16384) = w; }
        if (++spins > 4000000u) break;
      }
    }
    STMP(2, k * 8 + 2);
    __syncthreads();
    STMP(2, k * 8 + 3);
    if (wr && k <= 6) { u32 m1 = ~0u; asm volatile("" : "+v"(m1)); __builtin_amdgcn_raw_buffer_store_b128((u32x4){m1, m1, m1, m1}, rsG, reg, (k + 1) * 32 * TSTR, 16); }
    u32x4 tv = {0, 0, 0, 0};
    if (tk && k >= 3) tv = __builtin_amdgcn_raw_buffer_load_b128(rsG, tko, (k - 2) * 32 * TSTR, 16);
    if (active) {
      f32x4 acc0, acc1;
      STMP(2, k * 8 + 4);
      gather(ix, A, acc0, acc1);
      STMP(2, k * 8 + 5);
      if (nov) {
        *(u32x4*)(stage + lane * 16) = (u32x4){0, 0, 0, 0};
        if (lane < 16) *(u32x4*)(stage + 1024 + lane * 16) = (u32x4){0, 0, 0, 0};
        for (u32 i = lane; i < nov; i += 64) {
          const u32 e = ov2[i < c0 ? i : EE - 1 - (i - c0)];
          const u32x2 row = *(const u32x2*)(lds + (e & 0xffff) * 8);
          LAS float* p = (LAS float*)(stage + (e >> 16) * 16);
          __builtin_amdgcn_ds_faddf(p, bflo(row.x), 0, 0, false);
          __builtin_amdgcn_ds_faddf(p + 1, bfhi(row.x), 0, 0, false);
          __builtin_amdgcn_ds_faddf(p + 2, bflo(row.y), 0, 0, false);
          __builtin_amdgcn_ds_faddf(p + 3, bfhi(row.y), 0, 0, false);
        }
        __builtin_amdgcn_wave_barrier();
        acc0 += *(const f32x4*)(stage + n0 * 16);
        acc1 += *(const f32x4*)(stage + n1 * 16);
        asm volatile("s_waitcnt lgkmcnt(0)" ::: "memory");
      }
      u32x2 g0 = {pk_bf16(acc0[0] * dis0, acc0[1] * dis0), pk_bf16(acc0[2] * dis0, acc0[3] * dis0)};
      u32x2 g1 = {pk_bf16(acc1[0] * dis1, acc1[1] * dis1), pk_bf16(acc1[2] * dis1, acc1[3] * dis1)};
      g0.x = min(g0.x, 0xfffffffeu); g0.y = min(g0.y, 0xfffffffeu); g1.x = min(g1.x, 0xfffffffeu); g1.y = min(g1.y, 0xfffffffeu);
      *(u32x2*)(stage + n0 * 8) = g0;
      if (lane < 16) *(u32x2*)(stage + n1 * 8) = g1;
      __builtin_amdgcn_wave_barrier();
      const u32x4 gv = *(const u32x4*)(stage + lane * 16);
      asm volatile("s_waitcnt vmcnt(0)" ::: "memory");
      if (lane < 40) __builtin_amdgcn_raw_buffer_store_b128(gv, rsG, reg, k * 32 * TSTR, 16);
    }
    if (tk && k >= 3) token(tv, k - 2, L_TOK + (k - 2) * HS);
    STMP(2, k * 8 + 6);
    if (k == 7) break;
    asm volatile("s_waitcnt vmcnt(0)" ::: "memory");
    STMP(2, k * 8 + 7);
    __syncthreads();
    asm volatile("" ::: "memory");
#pragma unroll
    for (int i = 0; i < 5; i++) v[i] = __builtin_amdgcn_raw_buffer_load_b128(rsG, i < 4 ? lo + i * 16384 : lo4, (k * 32 + slice) * TSTR, 16);
  }
  if (b >= NN / 40) return;
  u32x4 tv = {0, 0, 0, 0};
  u32 dn = 0;
  const int node = b * 40 + wave * 4 + (lane & 3);
  if (tk) { dn = dis[node]; tv = __builtin_amdgcn_raw_buffer_load_b128(rsG, tko, 6 * 32 * TSTR, 16); }
  asm volatile("s_waitcnt lgkmcnt(0)\n s_barrier" ::: "memory");
  STMP(3, 0);
#pragma unroll
  for (int i = 0; i < 4; i++)
    __builtin_amdgcn_global_load_lds((const GAS void*)(ws + O_W + (wave * 4 + i) * 1024 + lane * 16), (LAS void*)(lds + (wave * 4 + i) * 1024), 16, 0, 0);
  asm volatile("s_waitcnt vmcnt(4)" ::: "memory");
  if (tk) token(tv, 6, L_TK6);
  STMP(3, 1);
  asm volatile("s_waitcnt lgkmcnt(0)\n s_barrier" ::: "memory");
  if (tk) token(__builtin_amdgcn_raw_buffer_load_b128(rsG, tko, 7 * 32 * TSTR, 16), 7, L_TK6 + HS);
  STMP(3, 2);
  asm volatile("s_waitcnt vmcnt(0)" ::: "memory");
  __syncthreads();
  STMP(3, 3);
  if (tk) {
    const float d = __uint_as_float(dn << 16);
    const bool one = (lane & 28) == 0 || d == 0.f;
    attn_tile(lds, wave, one ? 1.f : d, one ? 1.f : 1.f / d,
              out + (size_t)node * 128 + ((lane >> 4) & 1) * 32 + ((lane >> 3) & 1) * 16 + ((lane >> 2) & 1) * 8 + (lane >> 5) * 4);
    STMP(3, 8);
  }
}

extern "C" void kernel_launch(void* const* d_in, const int* in_sizes, int n_in, void* d_out, int out_size, void* d_ws, size_t ws_size,
                              hipStream_t stream) {
  char* ws = (char*)d_ws;
  (void)hipFuncSetAttribute((const void*)k_main, hipFuncAttributeMaxDynamicSharedMemorySize, LDS_MAIN);
  k_prep<<<NW1 + NB, 1024, 0, stream>>>((const u16*)d_in[0], (const int*)d_in[1], (const int*)d_in[2], (const u16*)d_in[3], ws);
  k_sched<<<2 * NB + 2, 1024, 0, stream>>>((const u16*)d_in[3], (const u16*)d_in[4], (const u16*)d_in[5], (const u16*)d_in[6],
                                           (const u16*)d_in[7], (const u16*)d_in[8], ws);
  k_main<<<256, 1024, LDS_MAIN, stream>>>((const u16*)d_in[0], (const u16*)d_in[3], ws, (u16*)d_out);
}
```

```cpp
#include <hip/hip_runtime.h>
#include <stdint.h>

typedef unsigned short u16;
typedef unsigned u32;
typedef u32 u32x2 __attribute__((ext_vector_type(2)));
typedef u32 u32x4 __attribute__((ext_vector_type(4)));
typedef u32 u32x8 __attribute__((ext_vector_type(8)));
typedef u32 u32x16 __attribute__((ext_vector_type(16)));
typedef float f32x2 __attribute__((ext_vector_type(2)));
typedef float f32x4 __attribute__((ext_vector_type(4)));
typedef float f32x16 __attribute__((ext_vector_type(16)));
typedef __bf16 bf16x2 __attribute__((ext_vector_type(2)));
typedef __bf16 bf16x8 __attribute__((ext_vector_type(8)));
#define LAS __attribute__((address_space(3)))
#define GAS __attribute__((address_space(1)))

constexpr int NN = 10000, EE = 320000, NB = 125, TSTR = 81920, TB = 80000;
constexpr int NW1 = 128, EPW = 2500, CAP1 = 48;
constexpr size_t O_G = 0;
constexpr size_t O_CELL = O_G + (size_t)8 * 32 * TSTR;
constexpr size_t O_OV1C = O_CELL + (size_t)NB * NW1 * CAP1 * 4;
constexpr size_t O_OV1 = O_OV1C + 512;
constexpr size_t O_IMG = O_OV1 + (size_t)EE * 4;
constexpr size_t O_META = O_IMG + (size_t)NB * 24 * 256;
constexpr size_t O_OV2C = O_META + (size_t)NB * 768;
constexpr size_t O_W = O_OV2C + 1024;
constexpr size_t O_FLG = O_W + 65536 + 2048;
constexpr int NREP = 8;
constexpr int CODE = 34304;
#define SLP 24
constexpr size_t O_OV2 = O_FLG + 256 + NREP * 2048;

__device__ __forceinline__ u32 pk_bf16(float a, float b) {
  f32x2 v = {a, b};
  return __builtin_bit_cast(u32, __builtin_convertvector(v, bf16x2));
}
__device__ __forceinline__ float bflo(u32 w) { return __uint_as_float(w << 16); }
__device__ __forceinline__ float bfhi(u32 w) { return __uint_as_float(w & 0xffff0000u); }
__device__ __forceinline__ f32x16 mfma32(u32x4 a, u32x4 b, f32x16 c) {
  return __builtin_amdgcn_mfma_f32_32x32x16_bf16(__builtin_bit_cast(bf16x8, a), __builtin_bit_cast(bf16x8, b), c, 0, 0, 0);
}
template <int CTRL> __device__ __forceinline__ u32 dpp(u32 v) { return __builtin_amdgcn_update_dpp(0u, v, CTRL, 0xf, 0xf, true); }
template <int CTRL> __device__ __forceinline__ float dppf(float v) { return __uint_as_float(dpp<CTRL>(__float_as_uint(v))); }
__device__ __forceinline__ float xor32f(float v) {
  auto r = __builtin_amdgcn_permlane32_swap(__float_as_uint(v), __float_as_uint(v), false, false);
  return __uint_as_float((threadIdx.x & 32) ? r[0] : r[1]);
}
__device__ __forceinline__ int ffbl(u32 v) { int r; asm("v_ffbl_b32 %0, %1" : "=v"(r) : "v"(v)); return r; }
__device__ __forceinline__ u32x4 pack8(const f32x16& a, int o) {
  return (u32x4){pk_bf16(a[o], a[o + 1]), pk_bf16(a[o + 2], a[o + 3]), pk_bf16(a[o + 4], a[o + 5]), pk_bf16(a[o + 6], a[o + 7])};
}

typedef __amdgpu_buffer_rsrc_t rsrc;
constexpr int DEV = 16;
constexpr u32 OOB = 0x80000000u;
__device__ __forceinline__ void st4(rsrc r, u32 o, u32x4 v) { __builtin_amdgcn_raw_buffer_store_b128(v, r, o, 0, DEV); }
__device__ __forceinline__ void st2(rsrc r, u32 o, u32x2 v) { __builtin_amdgcn_raw_buffer_store_b64(v, r, o, 0, DEV); }
__device__ __forceinline__ void st1(rsrc r, u32 o, u32 v) { __builtin_amdgcn_raw_buffer_store_b32(v, r, o, 0, DEV); }
__device__ __forceinline__ u32x4 ld4(rsrc r, u32 o) { return __builtin_amdgcn_raw_buffer_load_b128(r, o, 0, DEV); }
__device__ __forceinline__ u32x2 ld2(rsrc r, u32 o) { return __builtin_amdgcn_raw_buffer_load_b64(r, o, 0, DEV); }
__device__ __forceinline__ u32 ld1(rsrc r, u32 o) { return __builtin_amdgcn_raw_buffer_load_b32(r, o, 0, DEV); }
__device__ __forceinline__ void gst(u32* p, u32 v) { __hip_atomic_store((GAS u32*)p, v, __ATOMIC_RELAXED, __HIP_MEMORY_SCOPE_AGENT); }
__device__ __forceinline__ void drain() { asm volatile("s_waitcnt vmcnt(0)" ::: "memory"); }

constexpr u32 EMPTY = 80u << 16, TAGM = 0xff80c000u;
__device__ __forceinline__ void bin_edges(const u32x4 s4, const u32x4 d4, const u32x4 tg, const int b, char* lds, const rsrc rs) {
  const int tid = threadIdx.x;
  u32* img = (u32*)lds;
  u32* cnt = img + NB * CAP1;
  u32 so[2];
#pragma unroll
  for (int it = 0; it < 2; it++) {
    const u32 i = it * 1024 + tid, c = i / 12, p = i - c * 12, e = EMPTY + ((b & 4) << 14) + (b & 3) * 8 + (p & 7);
    so[it] = p < 8 ? O_CELL + (c * NW1 + b) * 128 + p * 16 : O_CELL + NB * NW1 * 128 + (c * NW1 + b) * 64 + (p - 8) * 16;
    if (i < 1500) ((u32x4*)img)[i] = (u32x4){e, e, e, e};
  }
  if (tid < 128) cnt[tid] = 0;
  __syncthreads();
  if (tid < EPW / 4) {
    u32 r[4], bk[4];
#pragma unroll
    for (int i = 0; i < 4; i++) { bk[i] = (d4[i] * 52429u) >> 22; r[i] = atomicAdd(&cnt[bk[i]], 1u); }
#pragma unroll
    for (int i = 0; i < 4; i++)
      if (r[i] < CAP1) img[bk[i] * CAP1 + (r[i] < 32 ? (r[i] & 7) * 4 + (r[i] >> 3) : 28 + (r[i] & 7) * 2 + (r[i] >> 3))] = (s4[i] + 32) | ((d4[i] - bk[i] * 80) << 16);
    if (max(max(r[0], r[1]), max(r[2], r[3])) >= CAP1) {
#pragma unroll
      for (int i = 0; i < 4; i++)
        if (r[i] >= CAP1) { u32 o = atomicAdd(&cnt[127], 1u); st1(rs, O_OV1 + (b * EPW + o) * 4, s4[i] | (d4[i] << 14)); }
    }
  }
  __syncthreads();
  st4(rs, so[0], ((u32x4*)img)[tid] ^ tg);
  if (tid < 1500 - 1024) st4(rs, so[1], ((u32x4*)img)[1024 + tid] ^ tg);
  if (tid == 0) st1(rs, O_OV1C + b * 4, cnt[127]);
}

__device__ __forceinline__ void slice_x(const u32x4 v0, const u32x4 v1, const u32 d0, const u32 d1, const int t, char* lds, const rsrc rs) {
  const int tid = threadIdx.x;
#pragma unroll
  for (int it = 0; it < 2; it++) {
    const u32 i = it * 1024 + tid;
    if (i < 1280) {
      const u32x4 v = it ? v1 : v0;
      const float dn = __uint_as_float((it ? d1 : d0) << 16);
      char* p = lds + (i & 15) * 1312 + (i >> 4) * 8;
      u32 g[4];
#pragma unroll
      for (int q = 0; q < 4; q++) g[q] = pk_bf16(bflo(v[q]) * dn, bfhi(v[q]) * dn);
      *(u32x2*)p = (u32x2){g[0], g[1]};
      *(u32x2*)(p + 656) = (u32x2){g[2], g[3]};
    }
  }
  __syncthreads();
#pragma unroll
  for (int it = 0; it < 2; it++) {
    const u32 i = it * 1024 + tid;
    if (i < 1280) {
      const u32 sl = i / 40, o = i - sl * 40;
      st4(rs, O_G + sl * TSTR + t * 640 + o * 16, *(const u32x4*)(lds + sl * 656 + o * 16));
    }
  }
}

__device__ __forceinline__ void fold_weights(const u16* __restrict__ wa, const u16* __restrict__ wb, const u16* __restrict__ wr, const int role,
                                             char* lds, const rsrc rs) {
  const int tid = threadIdx.x, lane = tid & 63, w = tid >> 6, r = lane & 31, h = lane >> 5, db = w & 3, cb = w >> 2;
  constexpr int RS = 272, MB = 128 * RS;
  u32x4 t4[4];
#pragma unroll
  for (int i = 0; i < 4; i++) t4[i] = ((const u32x4*)(i < 2 ? wa : wb))[(i & 1) * 1024 + tid];
#pragma unroll
  for (int i = 0; i < 4; i++) *(u32x4*)(lds + (i >> 1) * MB + ((i & 1) * 64 + (tid >> 4)) * RS + (tid & 15) * 16) = t4[i];
  __syncthreads();
  const bool isM = role == 0;
  f32x16 acc = {}, act = {};
  if (isM) {
#pragma unroll
    for (int j = 0; j < 8; j++) {
      const u32x4 a = *(const u32x4*)(lds + (db * 32 + r) * RS + j * 32 + h * 16);
      const u32x4 k = *(const u32x4*)(lds + MB + (cb * 32 + r) * RS + j * 32 + h * 16);
      acc = mfma32(a, k, acc);
      act = mfma32(k, a, act);
    }
  } else if (role == 1) {
#pragma unroll
    for (int eb = 0; eb < 4; eb++) {
      f32x16 t = {};
#pragma unroll
      for (int jj = 0; jj < 2; jj++) {
        const u32x4 a = *(const u32x4*)(lds + MB + (eb * 32 + r) * RS + cb * 64 + jj * 32 + h * 16);
        const int k = r - jj * 16 - h * 8;
        u32x4 id;
#pragma unroll
        for (int q = 0; q < 4; q++) id[q] = k == 2 * q ? 0x3f80u : k == 2 * q + 1 ? 0x3f800000u : 0u;
        t = mfma32(a, id, t);
      }
#pragma unroll
      for (int jj = 0; jj < 2; jj++) {
        const char* p = lds + (db * 32 + r) * RS + eb * 64 + jj * 32 + h * 8;
        const u32x2 lo = *(const u32x2*)p, hi = *(const u32x2*)(p + 16);
        acc = mfma32((u32x4){lo.x, lo.y, hi.x, hi.y}, pack8(t, jj * 8), acc);
      }
    }
  }
  if (role == 2) {
    float* y = (float*)(lds + 2 * MB);
    const u32 w2 = ((const u32*)wr)[lane];
    float sw = bflo(w2) + bfhi(w2);
#pragma unroll
    for (int o = 32; o; o >>= 1) sw += __shfl_xor(sw, o);
    const int e = tid >> 3, pt = tid & 7;
    float a = 0, c = 0;
#pragma unroll
    for (int q = 0; q < 8; q++) {
      const u32 m2 = *(const u32*)(lds + MB + e * RS + pt * 32 + q * 4), x2 = ((const u32*)wr)[pt * 8 + q];
      a += bflo(m2) + bfhi(m2);
      c += bflo(m2) * (bflo(x2) - sw * (1.f / 128)) + bfhi(m2) * (bfhi(x2) - sw * (1.f / 128));
    }
#pragma unroll
    for (int o = 4; o; o >>= 1) { a += __shfl_xor(a, o); c += __shfl_xor(c, o); }
    if (pt == 0) { y[e] = a; y[128 + e] = c; }
    __syncthreads();
    a = c = 0;
#pragma unroll
    for (int q = 0; q < 8; q++) {
      const u32 m2 = *(const u32*)(lds + e * RS + pt * 32 + q * 4);
      const int k = pt * 16 + q * 2;
      a += bflo(m2) * y[k] + bfhi(m2) * y[k + 1];
      c += bflo(m2) * y[128 + k] + bfhi(m2) * y[129 + k];
    }
#pragma unroll
    for (int o = 4; o; o >>= 1) { a += __shfl_xor(a, o); c += __shfl_xor(c, o); }
    if (pt == 0) { y[256 + e] = a; y[384 + e] = c; }
    __syncthreads();
    if (w == 0) {
      float ma = y[256 + lane] + y[320 + lane], mb = y[384 + lane] + y[448 + lane];
#pragma unroll
      for (int o = 32; o; o >>= 1) { ma += __shfl_xor(ma, o); mb += __shfl_xor(mb, o); }
      const int row = lane & 3, f0 = (lane >> 3) * 16 + ((lane >> 2) & 1) * 4;
      const float mean = (row == 2 ? ma : mb) * (1.f / 128);
      float v[8];
#pragma unroll
      for (int i = 0; i < 8; i++) {
        const int f = f0 + 8 * (i >> 2) + (i & 3);
        const float wf = __uint_as_float((u32)wr[f] << 16), yf = y[(row | 2) * 128 + f] - mean;
        v[i] = row == 0 ? 1.f : row == 1 ? wf : yf;
      }
      st4(rs, O_W + 65536 + lane * 16, (u32x4){pk_bf16(v[0], v[1]), pk_bf16(v[2], v[3]), pk_bf16(v[4], v[5]), pk_bf16(v[6], v[7])});
      if (lane == 0) st1(rs, O_W + 65536 + 1024, __float_as_uint(sw * (1.f / 128)));
    }
    return;
  }
  __syncthreads();
  float* pc = (float*)lds;
  float* pd = pc + 1024;
  float* md = pd + 1024;
  float s = 0, st = 0;
#pragma unroll
  for (int i = 0; i < 16; i++) { s += acc[i]; st += act[i]; }
  pc[(db * 2 + h) * 128 + cb * 32 + r] = s;
  pd[(cb * 2 + h) * 128 + db * 32 + r] = st;
  __syncthreads();
  if (tid < 128) { float a = 0;
#pragma unroll
    for (int i = 0; i < 8; i++) a += pd[i * 128 + tid];
    md[tid] = a * (1.f / 128); }
  float mc = 0;
#pragma unroll
  for (int i = 0; i < 8; i++) mc += pc[i * 128 + cb * 32 + r];
  mc *= 1.f / 128;
  __syncthreads();
  float tm = md[lane] + md[lane + 64];
#pragma unroll
  for (int o = 32; o; o >>= 1) tm += __shfl_xor(tm, o);
  tm *= 1.f / 128;
  const float sc = isM ? 0.08838834764831845f * 1.4426950408889634f : 1.f;
#pragma unroll
  for (int i = 0; i < 16; i++) acc[i] = (acc[i] - mc - md[db * 32 + (i & 3) + 8 * (i >> 2) + 4 * h] + tm) * sc;
  const u32 o = O_W + (isM ? 0 : 32768) + ((cb * 8 + db * 2) * 64 + lane) * 16;
  st4(rs, o, pack8(acc, 0));
  st4(rs, o + 1024, pack8(acc, 8));
}

template <int R> struct SchedRound {
  static __device__ __forceinline__ void run(const u32* E, u32& F, u32& L, u32 obase) {
    SchedRound<R - 1>::run(E, F, L, obase);
    constexpr int r = R - 1;
    if (r > 0) {
      constexpr int z = __builtin_ctz(r | 32);
      if (z == 0) F = dpp<0xB1>(F);
      else if (z == 1) F = dpp<0x4E>(F);
      else if (z == 2) F = dpp<0x141>(F);
      else if (z == 3) F = dpp<0x128>(F);
      else F = __builtin_amdgcn_ds_swizzle(F, 0x401F);
    }
    const u32 w = E[r >> 1];
    const u32 e = (r & 1) ? w >> 16 : w & 0xffffu;
    const u32 has = min(e, 1u);
    const int i = max(ffbl(F & L), ffbl(L));
    const u32 bit = has << (i & 31);
    F &= ~bit;
    L &= ~(bit | __builtin_bitreverse32(bit));
    *(LAS u16*)(uintptr_t)(obase + 2 * i) = (u16)e;
  }
};
template <> struct SchedRound<0> { static __device__ __forceinline__ void run(const u32*, u32&, u32&, u32) {} };

constexpr int ES = 72;
constexpr int S_CNT = 82176, S_IA = S_CNT + 10496, S_IB = S_IA + 10496, S_M2 = S_IB + 10496, S_E1 = 0x1c000, S_E2 = 0x20000, S_O2 = 0x24000;
constexpr int S_OVC = S_O2 + 192 * ES, S_KEY = S_OVC + 16 + 128, S_P2N = S_KEY + 160, S_END = S_P2N + 80;
static_assert(S_M2 + 1024 == S_E1, "lds map");
__device__ __forceinline__ constexpr u32 round_of(u32 x) {
  const u32 g = x ^ (((x >> 2) & 1) * 3);
  u32 r = g ^ (g >> 1);
  r ^= r >> 2; r ^= r >> 4;
  return ((r >> 1) << 10) | ((r & 1) << 1);
}
__device__ __forceinline__ void marks(const rsrc rs, const u32 reg) {
  const u32x4 inv = {~0u, ~0u, ~0u, ~0u};
  const u32 tid = threadIdx.x, b = blockIdx.x, bq = b >> 1, t2 = tid - 256;
  __builtin_amdgcn_raw_buffer_store_b128(inv, rs, reg, 32 * TSTR, DEV);
  __builtin_amdgcn_raw_buffer_store_b128(inv, rs, reg, 64 * TSTR, DEV);
  if (tid < 320 && b < 2 * NB) {
    st4(rs, tid < ((b & 1) ? 128 : 256) ? O_IMG + bq * 6144 + (b & 1) * 4096 + tid * 16 : t2 < ((b & 1) ? 8 : 40) ? O_META + bq * 768 + (b & 1) * 640 + t2 * 16 : OOB, inv);
    st1(rs, tid == 0 ? O_OV2C + b * 4 : OOB, ~0u);
  }
}
__device__ __forceinline__ void arrive(const rsrc rs, const u32x2 tag) {
  const int lane = threadIdx.x & 63;
  st2(rs, lane < NREP ? O_FLG + 256 + lane * 2048 + blockIdx.x * 8 : OOB, tag);
}
__device__ __forceinline__ void all_arrived(const rsrc rs, const u32x2 tag) {
  const int lane = threadIdx.x & 63;
  const u32 o = O_FLG + 256 + (blockIdx.x & (NREP - 1)) * 2048 + lane * 32;
  for (int spin = 0; spin < 0x80000; spin++) {
    const u32x4 f0 = ld4(rs, o), f1 = ld4(rs, o + 16);
    const u32 bad = (f0.x ^ tag.x) | (f0.y ^ tag.y) | (f0.z ^ tag.x) | (f0.w ^ tag.y) | (f1.x ^ tag.x) | (f1.y ^ tag.y) | (f1.z ^ tag.x) | (f1.w ^ tag.y);
    if (__builtin_amdgcn_ballot_w64(bad != 0) == 0) break;
    asm volatile("" ::: "memory");
  }
}
__device__ __forceinline__ void first_table(const rsrc rs, const u32x2 tag, const int wave, const u32 slice) {
  if (wave < 4) return;
  all_arrived(rs, tag);
  u32 so = slice * TSTR + (wave - 4) * 1024;
  const u32 la = 256 + (wave - 4) * 1024, lo = (threadIdx.x & 63) * 16;
#define PIECE "buffer_load_dwordx4 %[lo], %[rs], %[so] offen sc1 lds\n s_add_u32 m0, m0, 0x3000\n s_add_u32 %[so], %[so], 0x3000\n"
  asm volatile("s_mov_b32 m0, %[la]\n s_cmp_ge_u32 %[la], 256 + 8 * 1024\n s_cbranch_scc1 .Lft%=\n" PIECE ".Lft%=:\n" PIECE PIECE PIECE PIECE PIECE PIECE
               : [so] "+s"(so) : [la] "s"(la), [lo] "v"(lo), [rs] "s"(rs) : "memory", "scc");
}
__device__ __forceinline__ void slotless(u32* ovc, u32* ov2, const int part, const u32 e) {
  const u32 o = atomicAdd(&ovc[0], 1u);
  if (o < 32) ovc[4 + o] = e; else gst(ov2 + (part ? EE + 31 - o : o - 32), e);
}
template <class SETUP>
__device__ __forceinline__ void schedule(SETUP& setup, const u16* __restrict__ dis, const u32* en, const u32x2 tag, const u32 slice, char* lds, char* ws,
                                         const rsrc rs) {
  u16* key = (u16*)(lds + S_KEY);
  unsigned char* p2n = (unsigned char*)(lds + S_P2N);
  u32* m2 = (u32*)(lds + S_M2);
  u32* ovc = (u32*)(lds + S_OVC);
  const int tid = threadIdx.x, lane = tid & 63, wave = __builtin_amdgcn_readfirstlane(tid >> 6);
  const int b = blockIdx.x >> 1, part = blockIdx.x & 1;
  const bool deep = __builtin_amdgcn_ballot_w64(en[4] < EMPTY) != 0;
  u32 r[6] = {}, ca[6] = {};
#pragma unroll
  for (int i = 0; i < 4; i++) { ca[i] = S_CNT + (en[i] >> 16) * 128 + (en[i] & 31) * 4; r[i] = atomicAdd((u32*)(lds + ca[i]), 1u); }
  if (deep) {
#pragma unroll
    for (int i = 4; i < 6; i++) { ca[i] = S_CNT + (en[i] >> 16) * 128 + (en[i] & 31) * 4; r[i] = atomicAdd((u32*)(lds + ca[i]), 1u); }
    if (en[5] < EMPTY && (tid & 7) == 7) ovc[1] = 1;
  }
  __syncthreads();

  u32 pre[4];
  const int node = tid >> 3, q8 = tid & 7;
  if (tid < 640) {
    const u32x4 c = ((const u32x4*)(lds + S_CNT))[tid];
    const u32 c0 = min(c.x, 8u), cc1 = min(c.y, 8u), c2 = min(c.z, 8u), c3 = min(c.w, 8u);
    const u32 tot = c0 + cc1 + c2 + c3;
    u32 s = tot, t;
    t = dpp<0x111>(s); s += q8 >= 1 ? t : 0;
    t = dpp<0x112>(s); s += q8 >= 2 ? t : 0;
    t = dpp<0x114>(s); s += q8 >= 4 ? t : 0;
    pre[0] = s - tot; pre[1] = pre[0] + c0; pre[2] = pre[1] + cc1; pre[3] = pre[2] + c2;
    if (q8 == 7) key[node] = (u16)((s << 7) | (127 - node));
  }
  __syncthreads();
  if (tid < 640) {
    const u32 mine = key[node];
    const u32* kp = (const u32*)key + q8 * 5;
    u32 n = 0;
#pragma unroll
    for (int i = 0; i < 5; i++) { const u32 k2 = kp[i]; n += ((k2 & 0xffff) > mine) + ((k2 >> 16) > mine); }
    n += dpp<0xB1>(n); n += dpp<0x4E>(n); n += dpp<0x141>(n);
    const u32 tile = n >> 4, col = n & 15, tl = tile - 3 * part;
    if (tl < 3u - part) {
      const int lim = 48 - 8 * ((tile + 1) >> 1);
      const u32 a0 = S_E1 | round_of((q8 * 4) ^ col) | ((tl * 64 + col) << 2) | (col << 30);
      u32x4 ia, ib;
#pragma unroll
      for (int i = 0; i < 4; i++) { ia[i] = (a0 ^ round_of(i)) + (pre[i] << 30); ib[i] = min(max(lim - (int)pre[i], 0), 8); }
      ((u32x4*)(lds + S_IA))[tid] = ia;
      ((u32x4*)(lds + S_IB))[tid] = ib;
    }
    if (q8 == 0) p2n[n] = (unsigned char)node;
  }
  drain();
  __syncthreads();
  if (wave == 15) arrive(rs, tag);
  u32* ov2 = (u32*)(ws + O_OV2) + (size_t)b * EE;
  u32 ia[6] = {}, ib[6] = {};
#pragma unroll
  for (int i = 0; i < 4; i++) { ia[i] = *(const u32*)(lds + ca[i] + (S_IA - S_CNT)); ib[i] = *(const u32*)(lds + ca[i] + (S_IB - S_CNT)); }
  if (deep) {
#pragma unroll
    for (int i = 4; i < 6; i++) { ia[i] = *(const u32*)(lds + ca[i] + (S_IA - S_CNT)); ib[i] = *(const u32*)(lds + ca[i] + (S_IB - S_CNT)); }
  }
  u32 rest = 0;
#pragma unroll
  for (int i = 0; i < 6; i++) {
    if (i < 4 || deep) {
      const u32 t = ia[i] + (r[i] << 30);
      const u32 a = t ^ ((u32)((int)t >> 31) & (0x80000040u | round_of(16))) ^ ((u32)((int)(t << 1) >> 31) & 0x40000080u);
      const bool fast = r[i] < min(ib[i], 4u);
      if (fast) *(LAS u16*)(uintptr_t)a = (u16)en[i];
      if (!fast && ia[i]) { rest |= 1u << i; ia[i] = a; }
    }
  }
  if (rest) {
#pragma unroll
    for (int i = 0; i < 6; i++) {
      if (rest >> i & 1) {
        const u32 a = ia[i];
        if (r[i] < ib[i]) { *(LAS u16*)(uintptr_t)(a + S_E2 - S_E1) = (u16)en[i]; atomicOr(&m2[(a >> 2) & 255], 1u << (((a >> 9) & 30) | ((a >> 1) & 1))); }
        else slotless(ovc, ov2, part, en[i]);
      }
    }
  }
  const bool more = part == 0 && ovc[1];
  if (more) {
    all_arrived(rs, tag);
    for (int w = 0; w < NW1; w++) {
      const u32 n = ld1(rs, O_OV1C + w * 4);
      for (u32 i = tid; i < n; i += 1024) {
        const u32 e = ld1(rs, O_OV1 + (w * EPW + i) * 4), d = e >> 14;
        if (d - b * 80 < 80u) slotless(ovc, ov2, 0, ((e & 0x3fff) + 32) | ((d - b * 80) << 16));
      }
    }
  }
  drain();
  __syncthreads();
  if (wave >= 3 - part) setup();
  first_table(rs, tag, wave, slice);
  if (wave < 3 - part) {
    __builtin_amdgcn_s_setprio(3);
    const int tile = wave + 3 * part, S = 6 - ((tile + 1) >> 1);
    const u32 row = wave * 64 + lane, hh = lane >> 5;
    u32 E[16];
#pragma unroll
    for (int i = 0; i < 16; i++) E[i] = *(const u32*)(lds + S_E1 + i * 1024 + row * 4);
    const u32 full = (1u << (2 * S)) - 1;
    u32 L = full | __builtin_bitreverse32(full);
    u32 F = L & ~(((lane ^ hh) & 1) ? 0u : 1u << ((lane & 31) >> 1));
    const u32 ob = S_O2 + row * ES + 8;
    SchedRound<32>::run(E, F, L, ob);
    u32 m = m2[row];
    while (m) {
      const int rr = ffbl(m);
      m &= m - 1;
      const int i = 16 + ffbl(L >> 16);
      const u32 bit = 1u << i;
      L &= ~(bit | __builtin_bitreverse32(bit));
      *(LAS u16*)(uintptr_t)(ob + 2 * i) = *(const u16*)(lds + S_E2 + (rr >> 1) * 1024 + row * 4 + (rr & 1) * 2);
    }
    u32 o[6];
    u32x2 oa[3], oz[3];
#pragma unroll
    for (int s2 = 0; s2 < 3; s2++) { oa[s2] = *(LAS const u32x2*)(uintptr_t)(ob + s2 * 8); oz[s2] = *(LAS const u32x2*)(uintptr_t)(ob + 56 - s2 * 8); }
    asm volatile("" : "+v"(oa[0]), "+v"(oa[1]), "+v"(oa[2]), "+v"(oz[0]), "+v"(oz[1]), "+v"(oz[2]));
#pragma unroll
    for (int s2 = 0; s2 < 3; s2++) {
      const u32x2 a = oa[s2], c = oz[s2];
#pragma unroll
      for (int k = 0; k < 2; k++) {
        const int s = s2 * 2 + k;
        const u32 hi = k ? c.x : c.y;
        const u32 w = (k ? a.y : a.x) | ((hi >> 16) | (hi << 16));
        const u32 z = (4 * s + hh) * 0x10001u + 0x20000u;
        o[s] = max(w & 0xffffu, z & 0xffffu) | (max(w >> 16, z >> 16) << 16);
      }
    }
    const u32 ib = O_IMG + b * 6144;
    st4(rs, ib + part * 4096 + wave * 1024 + lane * 16, (u32x4){o[0], o[1], o[2], o[3]});
    if (tile == 0) st2(rs, ib + 3072 + lane * 8, (u32x2){o[4], o[5]});
    else if (tile < 3) st1(rs, ib + 3328 + tile * 256 + lane * 4, o[4]);
    __builtin_amdgcn_s_setprio(0);
  } else if (wave == 3) {
    if (part == 0) {
      const u32 n0 = p2n[lane], n1 = p2n[64 + (lane & 15)];
      st2(rs, O_META + b * 768 + lane * 8, (u32x2){n0 | ((u32)dis[b * 80 + n0] << 16), n1 | ((u32)dis[b * 80 + n1] << 16)});
    }
    st1(rs, lane < 32 ? O_META + b * 768 + 512 + part * 128 + lane * 4 : lane == 32 ? O_OV2C + blockIdx.x * 4 : OOB, ovc[lane < 32 ? 4 + lane : 0]);
  }
}

constexpr int L_TAB = 256, L_STG = L_TAB + TB, L_TOK = L_STG + 16 * 1280, HS = 10240 + 32, L_FUN = L_TOK + 6 * HS;
constexpr int L_TK6 = 65536 + 64, L_SCR = 90112, L_HND = L_SCR + 5120, LDS_MAIN = L_FUN + 1152 + 16;
static_assert((L_TK6 - L_TOK - 6 * HS) % 256 == 0 && L_TK6 + HS <= L_STG && L_TK6 + 2 * HS <= L_SCR && L_HND + 4096 <= L_TOK && LDS_MAIN <= 163840, "lds map");

#define CLOB "v28", "v29", "v30", "v31", "v32", "v33", "v34", "v35", "v36", "v37", "v38", "v39", "v40", "v41", "v42", "v43", "v44", "v45", "v46", "v47", "v52", \
  "v53", "v54", "v55", "v56", "v57", "v58", "v59", "v88", "v89", "v90", "v91", "v92", "v93", "v94", "v95", "v96", "v97", "v98", "v99", "v100", "v101", \
  "v102", "v103", "v104", "v105", "v114", "v115", "v116", "v117", "v118", "v119", "v120", "v121", "v122", "v123", "v126", "v127", "s60", "s61", "s62", \
  "s63", "s64", "s65", "s66", "s67", "s76", "s77"

constexpr int L_SB = L_TAB + TSTR;
static_assert(L_SB + 2 * 128 * 272 + 2048 <= LDS_MAIN && L_SB == S_CNT && S_END <= LDS_MAIN, "lds map");

__global__ __launch_bounds__(1024, 4) void k_main(const u16* __restrict__ x, const int* __restrict__ src, const int* __restrict__ dst,
                                                  const u16* __restrict__ dis, const u16* __restrict__ wq, const u16* __restrict__ wk,
                                                  const u16* __restrict__ wv, const u16* __restrict__ wo_, const u16* __restrict__ wr_,
                                                  char* ws, u16* __restrict__ out) {
  extern __shared__ __attribute__((aligned(16))) char lds[];
  asm volatile("" :: "s"(x), "s"(src), "s"(dst), "s"(dis), "s"(ws));
  u32x2 tag = *(const __attribute__((address_space(4))) u32x2*)(uintptr_t)(ws + O_FLG);
  const int tid = threadIdx.x, lane = tid & 63, wave = __builtin_amdgcn_readfirstlane(tid >> 6);
  const int b = blockIdx.x, xg = b & 7, j = b >> 3, slice = 4 * xg + (j & 3), range = j >> 2;
  const int bucket = range * 16 + wave;
  const bool active = bucket < NB;
  const rsrc rs = __builtin_amdgcn_make_buffer_rsrc(ws, 0, O_OV2, 0x00020000);
  const bool att = b < NN / 40, tk = wave < 10 && att, at = wave < 12 && att;
  const bool wr = active && lane < 40;
  const u32 lo = tid * 16, reg = wr ? slice * TSTR + bucket * 640 + lane * 16 : OOB;
  const bool bins = b < NW1, cuts = !bins && (att || (b >= 2 * NB + 2 && b < 2 * NB + 5));
  const int job = bins ? b : !cuts ? 0 : att ? b - NW1 : b - NW1 - 2;
  const u32x4 p0 = (bins ? (const u32x4*)src + job * (EPW / 4) : (const u32x4*)x + job * 1280)[bins ? min(tid, EPW / 4 - 1) : tid];
  const u32x4 p1 = (bins ? (const u32x4*)dst + job * (EPW / 4) : (const u32x4*)x + job * 1280 + 1024)[min(tid, bins ? EPW / 4 - 1 : 255)];
  const u32 q0 = dis[(bins ? 0 : job * 80) + (tid >> 4)], q1 = dis[(bins ? 0 : job * 80) + 64 + ((tid >> 4) & 15)];
  const int tile = wave < 8 ? wave : 4 + (wave >> 1), mode = wave < 8 || wave >= 12 ? 0 : 1 + (wave & 1);
  const u32 dr = dis[at ? b * 40 + tile * 4 + (lane & 3) : 0];
  u32 dummy = 0;
  if (wave == 15) {
    uint64_t pc;
    asm volatile("s_getpc_b64 %0" : "=s"(pc));
    asm volatile("global_load_dword %0, %1, %2" : "+v"(dummy) : "v"(min(j * 16 + (lane & 15), CODE / 128 - 1) * 128), "s"(pc));
  }
  if (att) {
#pragma unroll
    for (int it = 0; it < 3; it++) if (it * 1024 + tid < (S_E2 - S_CNT) / 16) ((u32x4*)(lds + S_CNT))[it * 1024 + tid] = (u32x4){0, 0, 0, 0};
    if (tid < (S_KEY - S_O2) / 16) ((u32x4*)(lds + S_O2))[tid] = (u32x4){0, 0, 0, 0};
  }
  asm volatile("" : "+s"(tag));
  tag.x = tag.x * 1664525u + 1013904223u;
  tag.y = tag.y * 22695477u + 0x9e3779b9u;
  const u32x4 tg = {tag.x & TAGM, (tag.x << 9 | tag.x >> 23) & TAGM, tag.y & TAGM, (tag.y << 9 | tag.y >> 23) & TAGM};
  if (bins) bin_edges(p0, p1, tg, b, lds, rs);
  else if (cuts) slice_x(p0, p1, q0, q1, job, lds, rs);
  u32x4 xv = {};
  if (tk) xv = ((const u32x4*)x)[b * 640 + tid];
  const u32 stg = L_STG + wave * 1280, wo = mode == 2 ? 32768 : 0;
  u32x16 va = {};
  u32x8 vc = {};
  u32x4 mk = {~0u, ~0u, ~0u, ~0u}, z4 = {0, 0, 0, 0}, A = {};
  auto setup = [&]() __attribute__((always_inline)) {
  const float d = __uint_as_float(dr << 16);
  const u32 h = lane >> 5, m = lane & 3, hop = (lane >> 2) & 7;
  const bool one = hop == 0 || d == 0.f;
  const float ds = one ? 1.f : d;
  va = (u32x16){(hop < 6 ? L_TOK : L_TK6 - 6 * HS) + hop * HS + h * 320 + (tile * 4 + m) * 8,
               L_FUN + (((lane >> 4) ^ hop) & 1 ? 128 : h * 64 + m * 16),
               lane * 16 + wo,
               L_SCR + tile * 512 + m * 32 + (hop & 1) * 16 + (hop >> 1) * 4,
               L_SCR + tile * 512 + (m * 2 + h) * 16,
               __float_as_uint(ds), __float_as_uint(1.f / ds), 0u, __float_as_uint(1e-5f * ds * ds),
               -(m & 1), -(m >> 1),
               m * 256 + ((lane >> 4) & 1) * 64 + ((lane >> 3) & 1) * 32 + (hop & 1) * 16 + h * 8,
               (u32)(L_TK6 + (lane >> 1) * 320 + tile * 32 + (lane & 1) * 16),
               (u32)((lane >> 1) * TSTR + b * 320 + tile * 32 + (lane & 1) * 16),
               lo,
               (u32)((tid / 20) * TSTR + b * 320 + (tid % 20) * 16)};
  vc = (u32x8){};
#pragma unroll
  for (int i = 0; i < 5; i++) {
    u32 o = (range + 1) * 10240 + i * 16384 + lo;
    o -= o >= TSTR ? TSTR : 0;
    vc[i] = o >= TB ? o - (TSTR - TB) : o;
  }
  vc[5] = lane * 4;
  {
    const int r = lane & 15;
    const u32 o1 = (r >> 2) ? 0u : (r & 1) ? 0x3f800000u : 0x00003f80u;
    A.x = A.z = (r & 2) ? 0u : o1;
    A.y = A.w = (r & 2) ? o1 : 0u;
  }
  asm volatile("" : "+{v[0:15]}"(va), "+{v[106:113]}"(vc), "+{v[60:63]}"(A));
  };
  if (att) {
    u32 en[6];
    drain();
    asm volatile("" :: "v"(dummy));
    if (!bins) asm volatile("s_sleep %0" :: "n"(SLP));
    for (int spin = 0; spin < 0x80000; spin++) {
      const u32x4 a0 = ld4(rs, O_CELL + (b >> 1) * NW1 * 128 + lo) ^ tg;
      const u32x2 a2 = ld2(rs, O_CELL + NB * NW1 * 128 + (b >> 1) * NW1 * 64 + tid * 8);
      en[0] = a0.x; en[1] = a0.y; en[2] = a0.z; en[3] = a0.w; en[4] = a2.x ^ ((lane & 1) ? tg.z : tg.x); en[5] = a2.y ^ ((lane & 1) ? tg.w : tg.y);
      if (__builtin_amdgcn_ballot_w64(((en[0] | en[1] | en[2] | en[3] | en[4] | en[5]) & TAGM) != 0) == 0) break;
      asm volatile("s_sleep 4" ::: "memory");
    }
    marks(rs, reg);
    schedule(setup, dis, en, tag, slice, lds, ws, rs);
  } else {
    marks(rs, reg);
    drain();
    asm volatile("" :: "v"(dummy));
    __syncthreads();
    if (wave == 15) arrive(rs, tag);
    if (b == 2 * NB) fold_weights(wq, wk, wr_, 0, lds + L_SB, rs);
    if (b == 2 * NB + 1) fold_weights(wv, wo_, wr_, 1, lds + L_SB, rs);
    if (b == 2 * NB + 5) fold_weights(wv, wo_, wr_, 2, lds + L_SB, rs);
    setup();
    first_table(rs, tag, wave, slice);
  }
  if (tid < 16) *(u32x4*)(lds + tid * 16) = (u32x4){0, 0, 0, 0};
  if (wave == 15) st2(rs, b == 0 && lane == 0 ? O_FLG : OOB, tag);
  __syncthreads();
  if (att && wave < 3 - (b & 1)) setup();
  if (tk) {
    char* p = lds + L_TOK + (tid & 15) * 640 + (tid >> 4) * 8;
    *(u32x2*)p = (u32x2){xv.x, xv.y};
    *(u32x2*)(p + 320) = (u32x2){xv.z, xv.w};
  }
  const int bk = active ? bucket : 0;
  for (int spin = 0; spin < 0x80000; spin++) {
    const u32 t = ld1(rs, lane < 5 ? O_IMG + bk * 6144 + (lane + (lane > 2)) * 1024 + 1020 : OOB);
    if (__builtin_amdgcn_ballot_w64(t == ~0u) == 0) break;
    asm volatile("" ::: "memory");
  }
  u32x16 ixa;
  u32x8 ixb;
  u32x2 mt, oc;
  u32 of;
  for (int spin = 0; spin < 0x80000; spin++) {
    const u32 ib = O_IMG + bk * 6144;
    const u32x4 t0 = ld4(rs, ib + lane * 16), t1 = ld4(rs, ib + 1024 + lane * 16), t2 = ld4(rs, ib + 2048 + lane * 16);
    const u32x4 t3 = ld4(rs, ib + 4096 + lane * 16), t4 = ld4(rs, ib + 5120 + lane * 16);
    const u32x2 u0 = ld2(rs, ib + 3072 + lane * 8);
    const u32 u1 = ld1(rs, ib + 3584 + lane * 4), u2 = ld1(rs, ib + 3840 + lane * 4);
    mt = ld2(rs, O_META + bk * 768 + lane * 8);
    of = ld1(rs, O_META + bk * 768 + 512 + lane * 4);
    oc = ld2(rs, O_OV2C + bk * 8);
    ixa = (u32x16){t0.x, t0.y, t0.z, t0.w, u0.x, u0.y, t1.x, t1.y, t1.z, t1.w, u1, t2.x, t2.y, t2.z, t2.w, u2};
    ixb = (u32x8){t3.x, t3.y, t3.z, t3.w, t4.x, t4.y, t4.z, t4.w};
    u32 mx = max(max(max(mt.x, mt.y), of), max(oc.x, oc.y));
#pragma unroll
    for (int i = 0; i < 16; i++) mx = max(mx, ixa[i]);
#pragma unroll
    for (int i = 0; i < 8; i++) mx = max(mx, ixb[i]);
    if (__builtin_amdgcn_ballot_w64(mx == ~0u) == 0) break;
    asm volatile("" ::: "memory");
  }
  const u32 ca = __builtin_amdgcn_readfirstlane(oc.x), cb = __builtin_amdgcn_readfirstlane(oc.y);
  const u32 c0 = 64 + max(ca, 32u) - 32, nov = ca + cb ? c0 + max(cb, 32u) - 32 : 0;
  const u32* ov2 = (const u32*)(ws + O_OV2) + (size_t)bk * EE - 64;
  const u32 n0 = mt.x & 0xffff, n1 = mt.y & 0xffff;
  u32x8 vb = {__float_as_uint(bfhi(mt.x) * bfhi(mt.x)), __float_as_uint(bfhi(mt.y) * bfhi(mt.y)), stg + n0 * 8, stg + (lane < 16 ? n1 * 8 : 640 + lane * 8),
              stg + lane * 16, reg, wr ? L_TAB + bucket * 640 + lane * 16 : stg + lane * 16,
              of};
  u32x2 vd = {stg + n0 * 16, stg + n1 * 16};
  const uint64_t pw = (uint64_t)ws, po = (uint64_t)ov2, pt = (uint64_t)(out + (size_t)(b * 40 + tile * 4) * 128), pg = pw + O_G + (size_t)7 * 32 * TSTR;
  u32x16 sa = {(u32)pw, (u32)(pw >> 32), (u32)O_OV2, 0x00020000u, 32u * TSTR, 1u, active, tk, at, nov, c0, (u32)slice * TSTR,
               (u32)po, (u32)(po >> 32), (u32)mode, L_HND + (tile & 1) * 2048 - wo};
  u32x8 sb = {(u32)pt, (u32)(pt >> 32), (u32)pg, (u32)(pg >> 32), __float_as_uint(1.f / 128), 0x7fffffffu, 0xf0000000u, 0xf0000000u};
  const uint64_t pd = 0;
  u32x8 sc = {stg, nov - 1, EE - 1 + 64 + c0, wave < 6, att, wave == 12, (u32)pd, (u32)(pd >> 32)};
#pragma unroll
  for (int i = 0; i < 16; i++) sa[i] = __builtin_amdgcn_readfirstlane(sa[i]);
#pragma unroll
  for (int i = 0; i < 8; i++) { sb[i] = __builtin_amdgcn_readfirstlane(sb[i]); sc[i] = __builtin_amdgcn_readfirstlane(sc[i]); }
  asm volatile(
  "s_waitcnt vmcnt(0) lgkmcnt(0)\n s_barrier\n"
  "s_mov_b32 s60, 0x80000\n"
  "v_lshrrev_b32 v112, 4, v14\n"
  "v_and_b32 v112, 63, v112\n"
  "v_cmp_gt_u32 vcc, s45, v112\n"
  "v_cmp_ne_u32 s[76:77], 0, v23\n"
  "s_nop 4\n"
  "s_and_b64 s[76:77], s[76:77], vcc\n"
  ".Lh_loop%=:\n"
  "s_add_u32 s67, s40, 5242880\n"
  "s_sub_u32 s63, s40, 2621440\n"
  "s_cmp_eq_u32 s41, 1\n"
  "s_cselect_b32 s62, 0, s43\n"
  "s_cmp_eq_u32 s41, 7\n"
  "s_cbranch_scc0 .Lh_t%=\n"
  "v_mov_b32 v15, v13\n"
  "s_mov_b32 s62, s44\n"
  ".Lh_t%=:\n"
  "s_cmp_eq_u32 s42, 0\n"
  "s_cbranch_scc1 .Lh_idle%=\n"
  "s_mov_b64 vcc, 0\n"
  "s_setprio 3\n"
  "s_cmp_eq_u32 s45, 0\n"
  "s_cbranch_scc1 .Lh_g%=\n"
  "v_mov_b32 v112, 0\n"
  "v_mov_b32 v113, 0\n"
  "v_mov_b32 v114, 0\n"
  "v_mov_b32 v115, 0\n"
  "ds_write_b128 v20, v[112:115]\n"
  "s_mov_b64 exec, 0xffff\n"
  "ds_write_b128 v20, v[112:115] offset:1024\n"
  "s_mov_b64 exec, -1\n"
  "v_mov_b32 v116, v23\n"
  "s_mov_b32 s61, 0\n"
  ".Lh_o%=:\n"
  "v_lshlrev_b32_sdwa v117, 3, v116 dst_sel:DWORD dst_unused:UNUSED_PAD src0_sel:DWORD src1_sel:WORD_0\n"
  "ds_read_b64 v[118:119], v117\n"
  "v_cmp_ne_u32 s[64:65], 0, v116\n"
  "v_lshrrev_b32 v116, 16, v116\n"
  "v_lshl_add_u32 v116, v116, 4, s68\n"
  "s_mov_b64 vcc, -1\n"
  "s_cmp_le_u32 s45, 64\n"
  "s_cbranch_scc1 .Lh_g%=\n"
  "v_lshrrev_b32 v112, 4, v14\n"
  "v_and_b32 v112, 63, v112\n"
  "v_add_u32 v112, s61, v112\n"
  "v_cmp_gt_u32 vcc, s45, v112\n"
  "s_waitcnt lgkmcnt(0)\n"
  "v_lshlrev_b32 v117, 16, v118\n"
  "v_and_b32 v118, 0xffff0000, v118\n"
  "v_lshlrev_b32 v113, 16, v119\n"
  "v_and_b32 v119, 0xffff0000, v119\n"
  "s_and_b64 exec, vcc, s[64:65]\n"
  "ds_add_f32 v116, v117\n"
  "ds_add_f32 v116, v118 offset:4\n"
  "ds_add_f32 v116, v113 offset:8\n"
  "ds_add_f32 v116, v119 offset:12\n"
  "s_mov_b64 exec, -1\n"
  "s_add_u32 s61, s61, 64\n"
  "s_cmp_ge_u32 s61, s45\n"
  "s_cbranch_scc1 .Lh_o2%=\n"
  "v_add_u32 v112, 64, v112\n"
  "v_min_u32 v112, s69, v112\n"
  "v_sub_u32 v113, s70, v112\n"
  "v_cmp_gt_u32 vcc, s46, v112\n"
  "v_cndmask_b32 v112, v113, v112, vcc\n"
  "v_lshlrev_b32 v112, 2, v112\n"
  "global_load_dword v116, v112, s[48:49] sc1\n"
  "s_waitcnt vmcnt(0)\n"
  "s_branch .Lh_o%=\n"
  ".Lh_o2%=:\n"
  "ds_read_b128 v[112:115], v124\n"
  "ds_read_b128 v[116:119], v125\n"
  "s_mov_b64 vcc, 0\n"
  ".Lh_g%=:\n"
  "s_setprio 3\n"
  "v_lshlrev_b32_sdwa v104, 3, v64 dst_sel:DWORD dst_unused:UNUSED_PAD src0_sel:DWORD src1_sel:WORD_0\n"
  "v_lshlrev_b32_sdwa v105, 3, v64 dst_sel:DWORD dst_unused:UNUSED_PAD src0_sel:DWORD src1_sel:WORD_1\n"
  "ds_read_b64 v[88:89], v104\n"
  "ds_read_b64 v[90:91], v105\n"
  "v_lshlrev_b32_sdwa v104, 3, v65 dst_sel:DWORD dst_unused:UNUSED_PAD src0_sel:DWORD src1_sel:WORD_0\n"
  "v_lshlrev_b32_sdwa v105, 3, v65 dst_sel:DWORD dst_unused:UNUSED_PAD src0_sel:DWORD src1_sel:WORD_1\n"
  "ds_read_b64 v[92:93], v104\n"
  "ds_read_b64 v[94:95], v105\n"
  "v_lshlrev_b32_sdwa v104, 3, v66 dst_sel:DWORD dst_unused:UNUSED_PAD src0_sel:DWORD src1_sel:WORD_0\n"
  "v_lshlrev_b32_sdwa v105, 3, v66 dst_sel:DWORD dst_unused:UNUSED_PAD src0_sel:DWORD src1_sel:WORD_1\n"
  "ds_read_b64 v[96:97], v104\n"
  "ds_read_b64 v[98:99], v105\n"
  "v_lshlrev_b32_sdwa v104, 3, v67 dst_sel:DWORD dst_unused:UNUSED_PAD src0_sel:DWORD src1_sel:WORD_0\n"
  "v_lshlrev_b32_sdwa v105, 3, v67 dst_sel:DWORD dst_unused:UNUSED_PAD src0_sel:DWORD src1_sel:WORD_1\n"
  "ds_read_b64 v[100:101], v104\n"
  "ds_read_b64 v[102:103], v105\n"
  "s_waitcnt lgkmcnt(4)\n"
  "v_mfma_f32_16x16x32_bf16 v[52:55], v[60:63], v[88:91], 0\n"
  "v_lshlrev_b32_sdwa v104, 3, v68 dst_sel:DWORD dst_unused:UNUSED_PAD src0_sel:DWORD src1_sel:WORD_0\n"
  "v_lshlrev_b32_sdwa v105, 3, v68 dst_sel:DWORD dst_unused:UNUSED_PAD src0_sel:DWORD src1_sel:WORD_1\n"
  "ds_read_b64 v[88:89], v104\n"
  "ds_read_b64 v[90:91], v105\n"
  "v_mfma_f32_16x16x32_bf16 v[52:55], v[60:63], v[92:95], v[52:55]\n"
  "s_cbranch_vccz .Lh_oa%=\n"
  "v_lshlrev_b32 v117, 16, v118\n"
  "v_and_b32 v118, 0xffff0000, v118\n"
  "v_lshlrev_b32 v113, 16, v119\n"
  "v_and_b32 v119, 0xffff0000, v119\n"
  "s_mov_b64 exec, s[76:77]\n"
  "ds_add_f32 v116, v117\n"
  "ds_add_f32 v116, v118 offset:4\n"
  "ds_add_f32 v116, v113 offset:8\n"
  "ds_add_f32 v116, v119 offset:12\n"
  "s_mov_b64 exec, -1\n"
  ".Lh_oa%=:\n"
  "s_cmp_ge_u32 s41, 6\n"
  "s_cbranch_scc1 .Lh_v1%=\n"
  "buffer_store_dwordx4 v[24:27], v21, s[36:39], s67 offen sc1\n"
  ".Lh_v1%=:\n"
  "v_lshlrev_b32_sdwa v104, 3, v69 dst_sel:DWORD dst_unused:UNUSED_PAD src0_sel:DWORD src1_sel:WORD_0\n"
  "v_lshlrev_b32_sdwa v105, 3, v69 dst_sel:DWORD dst_unused:UNUSED_PAD src0_sel:DWORD src1_sel:WORD_1\n"
  "ds_read_b64 v[92:93], v104\n"
  "ds_read_b64 v[94:95], v105\n"
  "s_waitcnt lgkmcnt(4)\n"
  "v_mfma_f32_16x16x32_bf16 v[52:55], v[60:63], v[96:99], v[52:55]\n"
  "v_lshlrev_b32_sdwa v104, 3, v70 dst_sel:DWORD dst_unused:UNUSED_PAD src0_sel:DWORD src1_sel:WORD_0\n"
  "v_lshlrev_b32_sdwa v105, 3, v70 dst_sel:DWORD dst_unused:UNUSED_PAD src0_sel:DWORD src1_sel:WORD_1\n"
  "ds_read_b64 v[96:97], v104\n"
  "ds_read_b64 v[98:99], v105\n"
  "v_mfma_f32_16x16x32_bf16 v[52:55], v[60:63], v[100:103], v[52:55]\n"
  "s_cmp_eq_u32 s62, 0\n"
  "s_cbranch_scc1 .Lh_v2%=\n"
  "buffer_load_dwordx4 v[28:31], v15, s[36:39], s63 offen sc1\n"
  ".Lh_v2%=:\n"
  "s_cmp_eq_u32 s41, 7\n"
  "v_lshlrev_b32_sdwa v104, 3, v71 dst_sel:DWORD dst_unused:UNUSED_PAD src0_sel:DWORD src1_sel:WORD_0\n"
  "v_lshlrev_b32_sdwa v105, 3, v71 dst_sel:DWORD dst_unused:UNUSED_PAD src0_sel:DWORD src1_sel:WORD_1\n"
  "ds_read_b64 v[100:101], v104\n"
  "ds_read_b64 v[102:103], v105\n"
  "s_waitcnt lgkmcnt(4)\n"
  "v_mfma_f32_16x16x32_bf16 v[52:55], v[60:63], v[88:91], v[52:55]\n"
  "v_lshlrev_b32_sdwa v104, 3, v72 dst_sel:DWORD dst_unused:UNUSED_PAD src0_sel:DWORD src1_sel:WORD_0\n"
  "v_lshlrev_b32_sdwa v105, 3, v72 dst_sel:DWORD dst_unused:UNUSED_PAD src0_sel:DWORD src1_sel:WORD_1\n"
  "ds_read_b64 v[88:89], v104\n"
  "ds_read_b64 v[90:91], v105\n"
  "v_mfma_f32_16x16x32_bf16 v[52:55], v[60:63], v[92:95], v[52:55]\n"
  "s_cbranch_scc0 .Lh_v3%=\n"
  "s_mov_b32 s61, %[ow]+0\n"
  "buffer_load_dwordx4 v[32:35], v14, s[36:39], s61 offen sc1\n"
  ".Lh_v3%=:\n"
  "s_setprio 2\n"
  "v_lshlrev_b32_sdwa v104, 3, v73 dst_sel:DWORD dst_unused:UNUSED_PAD src0_sel:DWORD src1_sel:WORD_0\n"
  "v_lshlrev_b32_sdwa v105, 3, v73 dst_sel:DWORD dst_unused:UNUSED_PAD src0_sel:DWORD src1_sel:WORD_1\n"
  "ds_read_b64 v[92:93], v104\n"
  "ds_read_b64 v[94:95], v105\n"
  "s_waitcnt lgkmcnt(4)\n"
  "v_mov_b32_dpp v60, v60 row_ror:4 row_mask:0xf bank_mask:0xf\n"
  "v_mov_b32_dpp v61, v61 row_ror:4 row_mask:0xf bank_mask:0xf\n"
  "v_mov_b32_dpp v62, v62 row_ror:4 row_mask:0xf bank_mask:0xf\n"
  "v_mov_b32_dpp v63, v63 row_ror:4 row_mask:0xf bank_mask:0xf\n"
  "s_nop 1\n"
  "v_mfma_f32_16x16x32_bf16 v[52:55], v[60:63], v[96:99], v[52:55]\n"
  "v_lshlrev_b32_sdwa v104, 3, v74 dst_sel:DWORD dst_unused:UNUSED_PAD src0_sel:DWORD src1_sel:WORD_0\n"
  "v_lshlrev_b32_sdwa v105, 3, v74 dst_sel:DWORD dst_unused:UNUSED_PAD src0_sel:DWORD src1_sel:WORD_1\n"
  "ds_read_b64 v[96:97], v104\n"
  "ds_read_b64 v[98:99], v105\n"
  "v_mfma_f32_16x16x32_bf16 v[52:55], v[60:63], v[100:103], v[52:55]\n"
  "s_cbranch_vccz .Lh_ob%=\n"
  "ds_read_b128 v[112:115], v124\n"
  "ds_read_b128 v[116:119], v125\n"
  ".Lh_ob%=:\n"
  "s_cbranch_scc0 .Lh_v4%=\n"
  "s_mov_b32 s61, %[ow]+16384\n"
  "buffer_load_dwordx4 v[36:39], v14, s[36:39], s61 offen sc1\n"
  ".Lh_v4%=:\n"
  "v_lshlrev_b32_sdwa v104, 3, v75 dst_sel:DWORD dst_unused:UNUSED_PAD src0_sel:DWORD src1_sel:WORD_0\n"
  "v_lshlrev_b32_sdwa v105, 3, v75 dst_sel:DWORD dst_unused:UNUSED_PAD src0_sel:DWORD src1_sel:WORD_1\n"
  "ds_read_b64 v[100:101], v104\n"
  "ds_read_b64 v[102:103], v105\n"
  "s_waitcnt lgkmcnt(4)\n"
  "v_mfma_f32_16x16x32_bf16 v[52:55], v[60:63], v[88:91], v[52:55]\n"
  "v_lshlrev_b32_sdwa v104, 3, v76 dst_sel:DWORD dst_unused:UNUSED_PAD src0_sel:DWORD src1_sel:WORD_0\n"
  "v_lshlrev_b32_sdwa v105, 3, v76 dst_sel:DWORD dst_unused:UNUSED_PAD src0_sel:DWORD src1_sel:WORD_1\n"
  "ds_read_b64 v[88:89], v104\n"
  "ds_read_b64 v[90:91], v105\n"
  "v_mfma_f32_16x16x32_bf16 v[52:55], v[60:63], v[92:95], v[52:55]\n"
  "s_cbranch_scc0 .Lh_v5%=\n"
  "s_mov_b32 s61, %[ow]+32768\n"
  "buffer_load_dwordx4 v[40:43], v14, s[36:39], s61 offen sc1\n"
  ".Lh_v5%=:\n"
  "v_lshlrev_b32_sdwa v104, 3, v77 dst_sel:DWORD dst_unused:UNUSED_PAD src0_sel:DWORD src1_sel:WORD_0\n"
  "v_lshlrev_b32_sdwa v105, 3, v77 dst_sel:DWORD dst_unused:UNUSED_PAD src0_sel:DWORD src1_sel:WORD_1\n"
  "ds_read_b64 v[92:93], v104\n"
  "ds_read_b64 v[94:95], v105\n"
  "s_waitcnt lgkmcnt(4)\n"
  "v_mfma_f32_16x16x32_bf16 v[52:55], v[60:63], v[96:99], v[52:55]\n"
  "v_lshlrev_b32_sdwa v104, 3, v78 dst_sel:DWORD dst_unused:UNUSED_PAD src0_sel:DWORD src1_sel:WORD_0\n"
  "v_lshlrev_b32_sdwa v105, 3, v78 dst_sel:DWORD dst_unused:UNUSED_PAD src0_sel:DWORD src1_sel:WORD_1\n"
  "ds_read_b64 v[96:97], v104\n"
  "ds_read_b64 v[98:99], v105\n"
  "v_mov_b32_dpp v60, v60 row_ror:4 row_mask:0xf bank_mask:0xf\n"
  "v_mov_b32_dpp v61, v61 row_ror:4 row_mask:0xf bank_mask:0xf\n"
  "v_mov_b32_dpp v62, v62 row_ror:4 row_mask:0xf bank_mask:0xf\n"
  "v_mov_b32_dpp v63, v63 row_ror:4 row_mask:0xf bank_mask:0xf\n"
  "s_nop 1\n"
  "v_mfma_f32_16x16x32_bf16 v[52:55], v[60:63], v[100:103], v[52:55]\n"
  "s_cbranch_scc0 .Lh_v6%=\n"
  "s_mov_b32 s61, %[ow]+49152\n"
  "buffer_load_dwordx4 v[44:47], v14, s[36:39], s61 offen sc1\n"
  ".Lh_v6%=:\n"
  "s_setprio 1\n"
  "v_lshlrev_b32_sdwa v104, 3, v79 dst_sel:DWORD dst_unused:UNUSED_PAD src0_sel:DWORD src1_sel:WORD_0\n"
  "v_lshlrev_b32_sdwa v105, 3, v79 dst_sel:DWORD dst_unused:UNUSED_PAD src0_sel:DWORD src1_sel:WORD_1\n"
  "ds_read_b64 v[100:101], v104\n"
  "ds_read_b64 v[102:103], v105\n"
  "s_waitcnt lgkmcnt(4)\n"
  "v_mfma_f32_16x16x32_bf16 v[52:55], v[60:63], v[88:91], v[52:55]\n"
  "v_lshlrev_b32_sdwa v104, 3, v80 dst_sel:DWORD dst_unused:UNUSED_PAD src0_sel:DWORD src1_sel:WORD_0\n"
  "v_lshlrev_b32_sdwa v105, 3, v80 dst_sel:DWORD dst_unused:UNUSED_PAD src0_sel:DWORD src1_sel:WORD_1\n"
  "ds_read_b64 v[88:89], v104\n"
  "ds_read_b64 v[90:91], v105\n"
  "v_mfma_f32_16x16x32_bf16 v[52:55], v[60:63], v[92:95], v[52:55]\n"
  "s_cbranch_scc0 .Lh_v7%=\n"
  "s_mov_b32 s61, %[ow]+65536\n"
  "buffer_load_dwordx4 v[48:51], v2, s[36:39], s61 offen sc1\n"
  "s_mov_b32 s61, %[ow]+66560\n"
  "buffer_load_dword v7, off, s[36:39], s61 sc1\n"
  ".Lh_v7%=:\n"
  "v_lshlrev_b32_sdwa v104, 3, v81 dst_sel:DWORD dst_unused:UNUSED_PAD src0_sel:DWORD src1_sel:WORD_0\n"
  "v_lshlrev_b32_sdwa v105, 3, v81 dst_sel:DWORD dst_unused:UNUSED_PAD src0_sel:DWORD src1_sel:WORD_1\n"
  "ds_read_b64 v[92:93], v104\n"
  "ds_read_b64 v[94:95], v105\n"
  "s_waitcnt lgkmcnt(4)\n"
  "v_mfma_f32_16x16x32_bf16 v[52:55], v[60:63], v[96:99], v[52:55]\n"
  "v_lshlrev_b32_sdwa v104, 3, v82 dst_sel:DWORD dst_unused:UNUSED_PAD src0_sel:DWORD src1_sel:WORD_0\n"
  "v_lshlrev_b32_sdwa v105, 3, v82 dst_sel:DWORD dst_unused:UNUSED_PAD src0_sel:DWORD src1_sel:WORD_1\n"
  "ds_read_b64 v[96:97], v104\n"
  "ds_read_b64 v[98:99], v105\n"
  "v_mfma_f32_16x16x32_bf16 v[52:55], v[60:63], v[100:103], v[52:55]\n"
  "v_lshlrev_b32_sdwa v104, 3, v83 dst_sel:DWORD dst_unused:UNUSED_PAD src0_sel:DWORD src1_sel:WORD_0\n"
  "v_lshlrev_b32_sdwa v105, 3, v83 dst_sel:DWORD dst_unused:UNUSED_PAD src0_sel:DWORD src1_sel:WORD_1\n"
  "ds_read_b64 v[100:101], v104\n"
  "ds_read_b64 v[102:103], v105\n"
  "s_waitcnt lgkmcnt(4)\n"
  "v_mov_b32_dpp v60, v60 row_ror:4 row_mask:0xf bank_mask:0xf\n"
  "v_mov_b32_dpp v61, v61 row_ror:4 row_mask:0xf bank_mask:0xf\n"
  "v_mov_b32_dpp v62, v62 row_ror:4 row_mask:0xf bank_mask:0xf\n"
  "v_mov_b32_dpp v63, v63 row_ror:4 row_mask:0xf bank_mask:0xf\n"
  "s_nop 1\n"
  "v_mfma_f32_16x16x32_bf16 v[52:55], v[60:63], v[88:91], v[52:55]\n"
  "v_lshlrev_b32_sdwa v104, 3, v84 dst_sel:DWORD dst_unused:UNUSED_PAD src0_sel:DWORD src1_sel:WORD_0\n"
  "v_lshlrev_b32_sdwa v105, 3, v84 dst_sel:DWORD dst_unused:UNUSED_PAD src0_sel:DWORD src1_sel:WORD_1\n"
  "ds_read_b64 v[88:89], v104\n"
  "ds_read_b64 v[90:91], v105\n"
  "v_mfma_f32_16x16x32_bf16 v[52:55], v[60:63], v[92:95], v[52:55]\n"
  "s_setprio 0\n"
  "v_lshlrev_b32_sdwa v104, 3, v85 dst_sel:DWORD dst_unused:UNUSED_PAD src0_sel:DWORD src1_sel:WORD_0\n"
  "v_lshlrev_b32_sdwa v105, 3, v85 dst_sel:DWORD dst_unused:UNUSED_PAD src0_sel:DWORD src1_sel:WORD_1\n"
  "ds_read_b64 v[92:93], v104\n"
  "ds_read_b64 v[94:95], v105\n"
  "s_waitcnt lgkmcnt(4)\n"
  "v_mfma_f32_16x16x32_bf16 v[52:55], v[60:63], v[96:99], v[52:55]\n"
  "v_lshlrev_b32_sdwa v104, 3, v86 dst_sel:DWORD dst_unused:UNUSED_PAD src0_sel:DWORD src1_sel:WORD_0\n"
  "v_lshlrev_b32_sdwa v105, 3, v86 dst_sel:DWORD dst_unused:UNUSED_PAD src0_sel:DWORD src1_sel:WORD_1\n"
  "ds_read_b64 v[96:97], v104\n"
  "ds_read_b64 v[98:99], v105\n"
  "v_mfma_f32_16x16x32_bf16 v[52:55], v[60:63], v[100:103], v[52:55]\n"
  "v_lshlrev_b32_sdwa v104, 3, v87 dst_sel:DWORD dst_unused:UNUSED_PAD src0_sel:DWORD src1_sel:WORD_0\n"
  "v_lshlrev_b32_sdwa v105, 3, v87 dst_sel:DWORD dst_unused:UNUSED_PAD src0_sel:DWORD src1_sel:WORD_1\n"
  "ds_read_b64 v[100:101], v104\n"
  "ds_read_b64 v[102:103], v105\n"
  "s_waitcnt lgkmcnt(4)\n"
  "v_mov_b32_dpp v60, v60 row_ror:4 row_mask:0xf bank_mask:0xf\n"
  "v_mov_b32_dpp v61, v61 row_ror:4 row_mask:0xf bank_mask:0xf\n"
  "v_mov_b32_dpp v62, v62 row_ror:4 row_mask:0xf bank_mask:0xf\n"
  "v_mov_b32_dpp v63, v63 row_ror:4 row_mask:0xf bank_mask:0xf\n"
  "s_nop 1\n"
  "v_mfma_f32_16x16x32_bf16 v[56:59], v[60:63], v[88:91], 0\n"
  "v_mfma_f32_16x16x32_bf16 v[56:59], v[60:63], v[92:95], v[56:59]\n"
  "s_waitcnt lgkmcnt(0)\n"
  "v_mfma_f32_16x16x32_bf16 v[56:59], v[60:63], v[96:99], v[56:59]\n"
  "v_mfma_f32_16x16x32_bf16 v[56:59], v[60:63], v[100:103], v[56:59]\n"
  "s_setprio 0\n"
  "s_nop 2\n"
  "s_cmp_eq_u32 s45, 0\n"
  "s_cbranch_scc1 .Lh_e%=\n"
  "v_add_f32 v52, v52, v112\n"
  "v_add_f32 v53, v53, v113\n"
  "v_add_f32 v54, v54, v114\n"
  "v_add_f32 v55, v55, v115\n"
  "s_nop 0\n"
  "v_add_f32 v56, v56, v116\n"
  "v_add_f32 v57, v57, v117\n"
  "v_add_f32 v58, v58, v118\n"
  "v_add_f32 v59, v59, v119\n"
  ".Lh_e%=:\n"
  "v_mul_f32 v52, v16, v52\n"
  "v_mul_f32 v53, v16, v53\n"
  "v_mul_f32 v54, v16, v54\n"
  "v_mul_f32 v55, v16, v55\n"
  "v_cvt_pk_bf16_f32 v52, v52, v53\n"
  "v_cvt_pk_bf16_f32 v53, v54, v55\n"
  "v_min_u32 v52, -2, v52\n"
  "v_min_u32 v53, -2, v53\n"
  "ds_write_b64 v18, v[52:53]\n"
  "v_mul_f32 v56, v17, v56\n"
  "v_mul_f32 v57, v17, v57\n"
  "v_mul_f32 v58, v17, v58\n"
  "v_mul_f32 v59, v17, v59\n"
  "v_cvt_pk_bf16_f32 v56, v56, v57\n"
  "v_cvt_pk_bf16_f32 v57, v58, v59\n"
  "v_min_u32 v56, -2, v56\n"
  "v_min_u32 v57, -2, v57\n"
  "ds_write_b64 v19, v[56:57]\n"
  "ds_read_b128 v[120:123], v20\n"
  "s_waitcnt lgkmcnt(0)\n"
  "buffer_store_dwordx4 v[120:123], v21, s[36:39], s40 offen sc1\n"
  "s_waitcnt vmcnt(1)\n"
  "s_branch .Lh_k%=\n"
  ".Lh_idle%=:\n"
  "s_cmp_ge_u32 s41, 6\n"
  "s_cbranch_scc1 .Lh_v8%=\n"
  "buffer_store_dwordx4 v[24:27], v21, s[36:39], s67 offen sc1\n"
  ".Lh_v8%=:\n"
  "s_cmp_eq_u32 s62, 0\n"
  "s_cbranch_scc1 .Lh_v9%=\n"
  "buffer_load_dwordx4 v[28:31], v15, s[36:39], s63 offen sc1\n"
  ".Lh_v9%=:\n"
  "s_cmp_eq_u32 s41, 7\n"
  "s_cbranch_scc0 .Lh_v10%=\n"
  "s_mov_b32 s61, %[ow]+0\n"
  "buffer_load_dwordx4 v[32:35], v14, s[36:39], s61 offen sc1\n"
  ".Lh_v10%=:\n"
  "s_cbranch_scc0 .Lh_v11%=\n"
  "s_mov_b32 s61, %[ow]+16384\n"
  "buffer_load_dwordx4 v[36:39], v14, s[36:39], s61 offen sc1\n"
  ".Lh_v11%=:\n"
  "s_cbranch_scc0 .Lh_v12%=\n"
  "s_mov_b32 s61, %[ow]+32768\n"
  "buffer_load_dwordx4 v[40:43], v14, s[36:39], s61 offen sc1\n"
  ".Lh_v12%=:\n"
  "s_cbranch_scc0 .Lh_v13%=\n"
  "s_mov_b32 s61, %[ow]+49152\n"
  "buffer_load_dwordx4 v[44:47], v14, s[36:39], s61 offen sc1\n"
  ".Lh_v13%=:\n"
  "s_cbranch_scc0 .Lh_v14%=\n"
  "s_mov_b32 s61, %[ow]+65536\n"
  "buffer_load_dwordx4 v[48:51], v2, s[36:39], s61 offen sc1\n"
  "s_mov_b32 s61, %[ow]+66560\n"
  "buffer_load_dword v7, off, s[36:39], s61 sc1\n"
  ".Lh_v14%=:\n"
  "s_waitcnt vmcnt(0)\n"
  ".Lh_k%=:\n"
  "s_cmp_eq_u32 s62, 0\n"
  "s_cbranch_scc1 .Lh_n%=\n"
  ".Lh_tc%=:\n"
  "v_max3_u32 v126, v28, v29, v30\n"
  "v_max_u32 v126, v126, v31\n"
  "v_cmp_eq_u32 vcc, -1, v126\n"
  "s_cbranch_vccz .Lh_td%=\n"
  "s_sub_u32 s60, s60, 1\n"
  "s_cbranch_scc1 .Lh_td%=\n"
  "buffer_load_dwordx4 v[28:31], v15, s[36:39], s63 offen sc1\n"
  "s_waitcnt vmcnt(0)\n"
  "s_branch .Lh_tc%=\n"
  ".Lh_td%=:\n"
  "s_cmp_eq_u32 s41, 7\n"
  "s_cbranch_scc1 .Lh_tail%=\n"
  "s_mul_i32 s61, s41, 10272\n"
  "v_add_u32 v126, s61, v14\n"
  "v_add_u32 v126, 90464, v126\n"
  "ds_write_b128 v126, v[28:31]\n"
  ".Lh_n%=:\n"
  "s_cmp_eq_u32 s41, 7\n"
  "s_cbranch_scc1 .Lh_tail%=\n"
  "s_sleep 12\n"
  "s_barrier\n"
  "s_add_u32 s61, s40, s47\n"
  "buffer_load_dwordx4 v[32:35], v106, s[36:39], s61 offen sc1\n"
  "buffer_load_dwordx4 v[36:39], v107, s[36:39], s61 offen sc1\n"
  "buffer_load_dwordx4 v[40:43], v108, s[36:39], s61 offen sc1\n"
  "buffer_load_dwordx4 v[44:47], v109, s[36:39], s61 offen sc1\n"
  "s_cmp_eq_u32 s71, 0\n"
  "s_cbranch_scc1 .Lh_l4%=\n"
  "buffer_load_dwordx4 v[48:51], v110, s[36:39], s61 offen sc1\n"
  "s_add_u32 s41, s41, 1\n"
  "ds_write_b128 v22, v[120:123]\n"
  "s_waitcnt vmcnt(4)\n"
  "ds_write_b128 v106, v[32:35] offset:256\n"
  "v_max3_u32 v126, v32, v33, v34\n"
  "v_max_u32 v126, v126, v35\n"
  "s_waitcnt vmcnt(3)\n"
  "ds_write_b128 v107, v[36:39] offset:256\n"
  "v_max3_u32 v126, v36, v37, v126\n"
  "v_max3_u32 v126, v38, v39, v126\n"
  "s_waitcnt vmcnt(2)\n"
  "ds_write_b128 v108, v[40:43] offset:256\n"
  "v_max3_u32 v126, v40, v41, v126\n"
  "v_max3_u32 v126, v42, v43, v126\n"
  "s_waitcnt vmcnt(1)\n"
  "ds_write_b128 v109, v[44:47] offset:256\n"
  "v_max3_u32 v126, v44, v45, v126\n"
  "v_max3_u32 v126, v46, v47, v126\n"
  "s_waitcnt vmcnt(0)\n"
  "ds_write_b128 v110, v[48:51] offset:256\n"
  "v_max3_u32 v126, v48, v49, v126\n"
  "v_max3_u32 v126, v50, v51, v126\n"
  "s_branch .Lh_c%=\n"
  ".Lh_l4%=:\n"
  "s_add_u32 s41, s41, 1\n"
  "ds_write_b128 v22, v[120:123]\n"
  "s_waitcnt vmcnt(3)\n"
  "ds_write_b128 v106, v[32:35] offset:256\n"
  "v_max3_u32 v126, v32, v33, v34\n"
  "v_max_u32 v126, v126, v35\n"
  "s_waitcnt vmcnt(2)\n"
  "ds_write_b128 v107, v[36:39] offset:256\n"
  "v_max3_u32 v126, v36, v37, v126\n"
  "v_max3_u32 v126, v38, v39, v126\n"
  "s_waitcnt vmcnt(1)\n"
  "ds_write_b128 v108, v[40:43] offset:256\n"
  "v_max3_u32 v126, v40, v41, v126\n"
  "v_max3_u32 v126, v42, v43, v126\n"
  "s_waitcnt vmcnt(0)\n"
  "ds_write_b128 v109, v[44:47] offset:256\n"
  "v_max3_u32 v126, v44, v45, v126\n"
  "v_max3_u32 v126, v46, v47, v126\n"
  ".Lh_c%=:\n"
  "v_cmp_eq_u32 vcc, -1, v126\n"
  "s_cbranch_vccz .Lh_ok%=\n"
  ".Lh_r%=:\n"
  "s_mov_b32 s66, 0\n"
  "v_max3_u32 v126, v32, v33, v34\n"
  "v_max_u32 v126, v126, v35\n"
  "v_cmp_eq_u32 vcc, -1, v126\n"
  "s_cbranch_vccz .Lh_r0%=\n"
  "buffer_load_dwordx4 v[32:35], v106, s[36:39], s61 offen sc1\n"
  "s_or_b32 s66, s66, 1\n"
  ".Lh_r0%=:\n"
  "v_max3_u32 v126, v36, v37, v38\n"
  "v_max_u32 v126, v126, v39\n"
  "v_cmp_eq_u32 vcc, -1, v126\n"
  "s_cbranch_vccz .Lh_r1%=\n"
  "buffer_load_dwordx4 v[36:39], v107, s[36:39], s61 offen sc1\n"
  "s_or_b32 s66, s66, 2\n"
  ".Lh_r1%=:\n"
  "v_max3_u32 v126, v40, v41, v42\n"
  "v_max_u32 v126, v126, v43\n"
  "v_cmp_eq_u32 vcc, -1, v126\n"
  "s_cbranch_vccz .Lh_r2%=\n"
  "buffer_load_dwordx4 v[40:43], v108, s[36:39], s61 offen sc1\n"
  "s_or_b32 s66, s66, 4\n"
  ".Lh_r2%=:\n"
  "v_max3_u32 v126, v44, v45, v46\n"
  "v_max_u32 v126, v126, v47\n"
  "v_cmp_eq_u32 vcc, -1, v126\n"
  "s_cbranch_vccz .Lh_r3%=\n"
  "buffer_load_dwordx4 v[44:47], v109, s[36:39], s61 offen sc1\n"
  "s_or_b32 s66, s66, 8\n"
  ".Lh_r3%=:\n"
  "v_max3_u32 v126, v48, v49, v50\n"
  "v_max_u32 v126, v126, v51\n"
  "v_cmp_eq_u32 vcc, -1, v126\n"
  "s_cbranch_vccz .Lh_r4%=\n"
  "buffer_load_dwordx4 v[48:51], v110, s[36:39], s61 offen sc1\n"
  "s_or_b32 s66, s66, 16\n"
  ".Lh_r4%=:\n"
  "s_cmp_eq_u32 s66, 0\n"
  "s_cbranch_scc1 .Lh_ok%=\n"
  "s_waitcnt vmcnt(0)\n"
  "s_bitcmp0_b32 s66, 0\n"
  "s_cbranch_scc1 .Lh_w0%=\n"
  "ds_write_b128 v106, v[32:35] offset:256\n"
  ".Lh_w0%=:\n"
  "s_bitcmp0_b32 s66, 1\n"
  "s_cbranch_scc1 .Lh_w1%=\n"
  "ds_write_b128 v107, v[36:39] offset:256\n"
  ".Lh_w1%=:\n"
  "s_bitcmp0_b32 s66, 2\n"
  "s_cbranch_scc1 .Lh_w2%=\n"
  "ds_write_b128 v108, v[40:43] offset:256\n"
  ".Lh_w2%=:\n"
  "s_bitcmp0_b32 s66, 3\n"
  "s_cbranch_scc1 .Lh_w3%=\n"
  "ds_write_b128 v109, v[44:47] offset:256\n"
  ".Lh_w3%=:\n"
  "s_bitcmp0_b32 s66, 4\n"
  "s_cbranch_scc1 .Lh_w4%=\n"
  "ds_write_b128 v110, v[48:51] offset:256\n"
  ".Lh_w4%=:\n"
  "s_sub_u32 s60, s60, 1\n"
  "s_cbranch_scc1 .Lh_ok%=\n"
  "s_branch .Lh_r%=\n"
  ".Lh_ok%=:\n"
  "s_add_u32 s40, s40, 2621440\n"
  "s_waitcnt lgkmcnt(0)\n"
  "s_barrier\n"
  "s_branch .Lh_loop%=\n"
  ".Lh_tail%=:\n"
  "s_cmp_eq_u32 s72, 0\n"
  "s_cbranch_scc1 .Lh_dump%=\n"
  "s_barrier\n"
  "ds_write_b128 v14, v[32:35] offset:0\n"
  "ds_write_b128 v14, v[36:39] offset:16384\n"
  "ds_write_b128 v14, v[40:43] offset:32768\n"
  "ds_write_b128 v14, v[44:47] offset:49152\n"
  "s_cmp_eq_u32 s44, 0\n"
  "s_cbranch_scc0 .Lh_at%=\n"
  "s_cmp_eq_u32 s73, 0\n"
  "s_cbranch_scc1 .Lh_x%=\n"
  "v_lshrrev_b32 v126, 7, v2\n"
  "v_lshl_add_u32 v126, v126, 4, v2\n"
  "v_add_u32 v126, 162368, v126\n"
  "ds_write_b128 v126, v[48:51]\n"
  "v_mov_b32 v48, 0\n"
  "v_mov_b32 v49, 0\n"
  "v_mov_b32 v50, 0\n"
  "v_mov_b32 v51, 0\n"
  "v_mul_u32_u24 v126, 9, v2\n"
  "v_add_u32 v126, 162496, v126\n"
  "s_mov_b64 exec, 0xff\n"
  "ds_write_b128 v126, v[48:51]\n"
  "s_mov_b64 exec, -1\n"
  ".Lh_x%=:\n"
  "s_waitcnt lgkmcnt(0)\n"
  "s_barrier\n"
  ".Lh_dump%=:\n"
  "s_branch .Lat_e%=\n"
  ".Lh_at%=:\n"
  "ds_write_b128 v12, v[28:31]\n"
  "s_cmp_eq_u32 s50, 0\n"
  "s_cbranch_scc1 .Lh_f%=\n"
  "v_add_u32 v126, s51, v2\n"
  "v_mov_b32 v28, 0\n"
  "ds_write_b32 v126, v28 offset:1032\n"
  "s_setprio 3\n"
  "s_cmp_eq_u32 s50, 1\n"
  "s_cbranch_scc1 .Lh_go%=\n"
  "s_setprio 1\n"
  "s_branch .Lh_go%=\n"
  ".Lh_f%=:\n"
  "s_setprio 3\n"
  "s_cmp_lt_u32 s68, 85376\n"
  "s_cbranch_scc1 .Lh_go%=\n"
  "s_setprio 2\n"
  ".Lh_go%=:\n"
  "global_load_dwordx4 v[24:27], v13, s[54:55] sc1\n"
  "s_waitcnt lgkmcnt(0)\n"
  "s_barrier\n"
  "v_add_u32 v16, 2560, v0\n"
  "v_add_u32 v17, 5120, v0\n"
  "v_add_u32 v18, 7680, v0\n"
  "ds_read2_b64 v[32:35], v0 offset0:0 offset1:80\n"
  "ds_read_b128 v[80:83], v1 offset:0\n"
  "ds_read2_b64 v[36:39], v0 offset0:160 offset1:240\n"
  "ds_read_b128 v[84:87], v1 offset:144\n"
  "ds_read2_b64 v[40:43], v16 offset0:0 offset1:80\n"
  "ds_read_b128 v[88:91], v1 offset:288\n"
  "ds_read2_b64 v[44:47], v16 offset0:160 offset1:240\n"
  "ds_read_b128 v[92:95], v1 offset:432\n"
  "ds_read2_b64 v[48:51], v17 offset0:0 offset1:80\n"
  "ds_read_b128 v[96:99], v1 offset:576\n"
  "ds_read2_b64 v[52:55], v17 offset0:160 offset1:240\n"
  "ds_read_b128 v[100:103], v1 offset:720\n"
  "ds_read2_b64 v[56:59], v18 offset0:0 offset1:80\n"
  "ds_read_b128 v[104:107], v1 offset:864\n"
  "ds_read2_b64 v[60:63], v18 offset0:160 offset1:240\n"
  "ds_read_b128 v[108:111], v1 offset:1008\n"
  "ds_read_b128 v[64:67], v2 offset:0\n"
  "ds_read_b128 v[68:71], v2 offset:1024\n"
  "ds_read_b128 v[72:75], v2 offset:2048\n"
  "ds_read_b128 v[76:79], v2 offset:3072\n"
  "s_branch .Lat_c%=\n"
  ".Lat_p%=:\n"
  "global_load_dwordx4 v[24:27], v13, s[54:55] sc1\n"
  ".Lat_c%=:\n"
  "s_waitcnt vmcnt(0)\n"
  "v_max3_u32 v14, v24, v25, v26\n"
  "v_max_u32 v14, v14, v27\n"
  "s_sub_u32 s60, s60, 1\n"
  "s_cbranch_scc1 .Lat_g%=\n"
  "v_cmp_eq_u32 vcc, -1, v14\n"
  "s_cbranch_vccnz .Lat_p%=\n"
  ".Lat_g%=:\n"
  "ds_write_b128 v12, v[24:27] offset:10272\n"
  "s_mov_b64 exec, s[58:59]\n"
  "ds_read2_b64 v[32:35], v0 offset0:0 offset1:80\n"
  "ds_read2_b64 v[36:39], v0 offset0:160 offset1:240\n"
  "ds_read2_b64 v[40:43], v16 offset0:0 offset1:80\n"
  "ds_read2_b64 v[44:47], v16 offset0:160 offset1:240\n"
  "ds_read2_b64 v[48:51], v17 offset0:0 offset1:80\n"
  "ds_read2_b64 v[52:55], v17 offset0:160 offset1:240\n"
  "ds_read2_b64 v[56:59], v18 offset0:0 offset1:80\n"
  "ds_read2_b64 v[60:63], v18 offset0:160 offset1:240\n"
  "s_mov_b64 exec, -1\n"
  "s_cmp_eq_u32 s50, 2\n"
  "s_cbranch_scc1 .Lat_b%=\n"
  "s_waitcnt lgkmcnt(7)\n"
  "v_mfma_f32_16x16x32_bf16 v[112:115], v[80:83], v[32:35], 0\n"
  "v_mfma_f32_4x4x4_16b_bf16 v[116:119], v[32:33], v[32:33], 0\n"
  "v_mfma_f32_4x4x4_16b_bf16 v[120:123], v[34:35], v[34:35], 0\n"
  "s_waitcnt lgkmcnt(6)\n"
  "v_mfma_f32_16x16x32_bf16 v[112:115], v[84:87], v[36:39], v[112:115]\n"
  "v_mfma_f32_4x4x4_16b_bf16 v[116:119], v[36:37], v[36:37], v[116:119]\n"
  "v_mfma_f32_4x4x4_16b_bf16 v[120:123], v[38:39], v[38:39], v[120:123]\n"
  "s_waitcnt lgkmcnt(5)\n"
  "v_mfma_f32_16x16x32_bf16 v[112:115], v[88:91], v[40:43], v[112:115]\n"
  "v_mfma_f32_4x4x4_16b_bf16 v[116:119], v[40:41], v[40:41], v[116:119]\n"
  "v_mfma_f32_4x4x4_16b_bf16 v[120:123], v[42:43], v[42:43], v[120:123]\n"
  "s_waitcnt lgkmcnt(4)\n"
  "v_mfma_f32_16x16x32_bf16 v[112:115], v[92:95], v[44:47], v[112:115]\n"
  "v_mfma_f32_4x4x4_16b_bf16 v[116:119], v[44:45], v[44:45], v[116:119]\n"
  "v_mfma_f32_4x4x4_16b_bf16 v[120:123], v[46:47], v[46:47], v[120:123]\n"
  "s_waitcnt lgkmcnt(3)\n"
  "v_mfma_f32_16x16x32_bf16 v[112:115], v[96:99], v[48:51], v[112:115]\n"
  "v_mfma_f32_4x4x4_16b_bf16 v[116:119], v[48:49], v[48:49], v[116:119]\n"
  "v_mfma_f32_4x4x4_16b_bf16 v[120:123], v[50:51], v[50:51], v[120:123]\n"
  "s_waitcnt lgkmcnt(2)\n"
  "v_mfma_f32_16x16x32_bf16 v[112:115], v[100:103], v[52:55], v[112:115]\n"
  "v_mfma_f32_4x4x4_16b_bf16 v[116:119], v[52:53], v[52:53], v[116:119]\n"
  "v_mfma_f32_4x4x4_16b_bf16 v[120:123], v[54:55], v[54:55], v[120:123]\n"
  "s_waitcnt lgkmcnt(1)\n"
  "v_mfma_f32_16x16x32_bf16 v[112:115], v[104:107], v[56:59], v[112:115]\n"
  "v_mfma_f32_4x4x4_16b_bf16 v[116:119], v[56:57], v[56:57], v[116:119]\n"
  "v_mfma_f32_4x4x4_16b_bf16 v[120:123], v[58:59], v[58:59], v[120:123]\n"
  "s_waitcnt lgkmcnt(0)\n"
  "v_mfma_f32_16x16x32_bf16 v[112:115], v[108:111], v[60:63], v[112:115]\n"
  "v_mfma_f32_4x4x4_16b_bf16 v[116:119], v[60:61], v[60:61], v[116:119]\n"
  "v_mfma_f32_4x4x4_16b_bf16 v[120:123], v[62:63], v[62:63], v[120:123]\n"
  "v_mfma_f32_32x32x16_bf16 v[80:95], v[64:67], v[32:35], 0\n"
  "ds_read_b128 v[64:67], v2 offset:4096\n"
  "s_nop 2\n"
  "v_add_f32 v116, v116, v120\n"
  "v_add_f32 v117, v117, v121\n"
  "v_add_f32 v118, v118, v122\n"
  "v_add_f32 v119, v119, v123\n"
  "v_bfi_b32 v16, v9, v117, v116\n"
  "v_mfma_f32_32x32x16_bf16 v[80:95], v[68:71], v[36:39], v[80:95]\n"
  "ds_read_b128 v[68:71], v2 offset:5120\n"
  "v_bfi_b32 v17, v9, v119, v118\n"
  "v_bfi_b32 v12, v10, v17, v16\n"
  "v_mov_b32 v16, v12\n"
  "v_mfma_f32_32x32x16_bf16 v[80:95], v[72:75], v[40:43], v[80:95]\n"
  "ds_read_b128 v[72:75], v2 offset:6144\n"
  "v_permlane32_swap_b32 v12, v16\n"
  "v_add_f32 v12, v12, v16\n"
  "v_mfma_f32_32x32x16_bf16 v[80:95], v[76:79], v[44:47], v[80:95]\n"
  "ds_read_b128 v[76:79], v2 offset:7168\n"
  "v_mul_f32 v16, 0x3c000000, v112\n"
  "v_mul_f32 v16, v16, v16\n"
  "v_fma_f32 v12, v12, s56, -v16\n"
  "v_max_f32 v12, 0, v12\n"
  "s_waitcnt lgkmcnt(2)\n"
  "v_mfma_f32_32x32x16_bf16 v[80:95], v[64:67], v[48:51], v[80:95]\n"
  "ds_read_b128 v[64:67], v2 offset:8192\n"
  "v_add_f32 v12, v12, v8\n"
  "v_rsq_f32 v12, v12\n"
  "v_mov_b32 v1, v112\n"
  "v_fma_f32 v7, -v7, v112, v113\n"
  "v_mfma_f32_32x32x16_bf16 v[80:95], v[68:71], v[52:55], v[80:95]\n"
  "ds_read_b128 v[68:71], v2 offset:9216\n"
  "v_bfi_b32 v0, s57, v12, v5\n"
  "ds_write2_b32 v3, v0, v114 offset1:32\n"
  "ds_write_b32 v3, v115 offset:256\n"
  "s_waitcnt lgkmcnt(4)\n"
  "v_mfma_f32_32x32x16_bf16 v[80:95], v[72:75], v[56:59], v[80:95]\n"
  "ds_read_b128 v[72:75], v2 offset:10240\n"
  "v_mfma_f32_32x32x16_bf16 v[80:95], v[76:79], v[60:63], v[80:95]\n"
  "ds_read_b128 v[76:79], v2 offset:11264\n"
  "ds_read_b128 v[20:23], v4\n"
  "s_waitcnt lgkmcnt(5)\n"
  "v_mfma_f32_32x32x16_bf16 v[96:111], v[64:67], v[32:35], 0\n"
  "ds_read_b128 v[64:67], v2 offset:12288\n"
  "v_mfma_f32_32x32x16_bf16 v[96:111], v[68:71], v[36:39], v[96:111]\n"
  "ds_read_b128 v[68:71], v2 offset:13312\n"
  "s_waitcnt lgkmcnt(3)\n"
  "v_mfma_f32_32x32x16_bf16 v[96:111], v[72:75], v[40:43], v[96:111]\n"
  "ds_read_b128 v[72:75], v2 offset:14336\n"
  "v_mfma_f32_32x32x16_bf16 v[96:111], v[76:79], v[44:47], v[96:111]\n"
  "ds_read_b128 v[76:79], v2 offset:15360\n"
  "v_cvt_pk_bf16_f32 v24, v80, v81\n"
  "v_cvt_pk_bf16_f32 v25, v82, v83\n"
  "v_cvt_pk_bf16_f32 v26, v84, v85\n"
  "v_cvt_pk_bf16_f32 v27, v86, v87\n"
  "s_waitcnt lgkmcnt(2)\n"
  "v_mfma_f32_32x32x16_bf16 v[96:111], v[64:67], v[48:51], v[96:111]\n"
  "ds_read_b128 v[64:67], v2 offset:16384\n"
  "v_mfma_f32_32x32x16_bf16 v[112:127], v[32:35], v[24:27], 0\n"
  "v_cvt_pk_bf16_f32 v28, v88, v89\n"
  "v_cvt_pk_bf16_f32 v29, v90, v91\n"
  "v_cvt_pk_bf16_f32 v30, v92, v93\n"
  "v_cvt_pk_bf16_f32 v31, v94, v95\n"
  "v_mfma_f32_32x32x16_bf16 v[96:111], v[68:71], v[52:55], v[96:111]\n"
  "ds_read_b128 v[68:71], v2 offset:17408\n"
  "v_mfma_f32_32x32x16_bf16 v[112:127], v[36:39], v[28:31], v[112:127]\n"
  "s_waitcnt lgkmcnt(2)\n"
  "v_mfma_f32_32x32x16_bf16 v[96:111], v[72:75], v[56:59], v[96:111]\n"
  "ds_read_b128 v[72:75], v2 offset:18432\n"
  "v_mfma_f32_32x32x16_bf16 v[96:111], v[76:79], v[60:63], v[96:111]\n"
  "ds_read_b128 v[76:79], v2 offset:19456\n"
  "s_waitcnt lgkmcnt(2)\n"
  "v_mfma_f32_32x32x16_bf16 v[80:95], v[64:67], v[32:35], 0\n"
  "ds_read_b128 v[64:67], v2 offset:20480\n"
  "v_mfma_f32_32x32x16_bf16 v[80:95], v[68:71], v[36:39], v[80:95]\n"
  "ds_read_b128 v[68:71], v2 offset:21504\n"
  "s_waitcnt lgkmcnt(2)\n"
  "v_mfma_f32_32x32x16_bf16 v[80:95], v[72:75], v[40:43], v[80:95]\n"
  "ds_read_b128 v[72:75], v2 offset:22528\n"
  "v_mfma_f32_32x32x16_bf16 v[80:95], v[76:79], v[44:47], v[80:95]\n"
  "ds_read_b128 v[76:79], v2 offset:23552\n"
  "s_nop 0\n"
  "v_cvt_pk_bf16_f32 v24, v96, v97\n"
  "v_cvt_pk_bf16_f32 v25, v98, v99\n"
  "v_cvt_pk_bf16_f32 v26, v100, v101\n"
  "v_cvt_pk_bf16_f32 v27, v102, v103\n"
  "s_waitcnt lgkmcnt(2)\n"
  "v_mfma_f32_32x32x16_bf16 v[80:95], v[64:67], v[48:51], v[80:95]\n"
  "ds_read_b128 v[64:67], v2 offset:24576\n"
  "v_mfma_f32_32x32x16_bf16 v[112:127], v[40:43], v[24:27], v[112:127]\n"
  "v_cvt_pk_bf16_f32 v28, v104, v105\n"
  "v_cvt_pk_bf16_f32 v29, v106, v107\n"
  "v_cvt_pk_bf16_f32 v30, v108, v109\n"
  "v_cvt_pk_bf16_f32 v31, v110, v111\n"
  "v_mfma_f32_32x32x16_bf16 v[80:95], v[68:71], v[52:55], v[80:95]\n"
  "ds_read_b128 v[68:71], v2 offset:25600\n"
  "v_mfma_f32_32x32x16_bf16 v[112:127], v[44:47], v[28:31], v[112:127]\n"
  "s_waitcnt lgkmcnt(2)\n"
  "v_mfma_f32_32x32x16_bf16 v[80:95], v[72:75], v[56:59], v[80:95]\n"
  "ds_read_b128 v[72:75], v2 offset:26624\n"
  "v_mfma_f32_32x32x16_bf16 v[80:95], v[76:79], v[60:63], v[80:95]\n"
  "ds_read_b128 v[76:79], v2 offset:27648\n"
  "s_waitcnt lgkmcnt(2)\n"
  "v_mfma_f32_32x32x16_bf16 v[96:111], v[64:67], v[32:35], 0\n"
  "ds_read_b128 v[64:67], v2 offset:28672\n"
  "v_mfma_f32_32x32x16_bf16 v[96:111], v[68:71], v[36:39], v[96:111]\n"
  "ds_read_b128 v[68:71], v2 offset:29696\n"
  "s_waitcnt lgkmcnt(2)\n"
  "v_mfma_f32_32x32x16_bf16 v[96:111], v[72:75], v[40:43], v[96:111]\n"
  "ds_read_b128 v[72:75], v2 offset:30720\n"
  "v_mfma_f32_32x32x16_bf16 v[96:111], v[76:79], v[44:47], v[96:111]\n"
  "ds_read_b128 v[76:79], v2 offset:31744\n"
  "s_nop 0\n"
  "v_cvt_pk_bf16_f32 v24, v80, v81\n"
  "v_cvt_pk_bf16_f32 v25, v82, v83\n"
  "v_cvt_pk_bf16_f32 v26, v84, v85\n"
  "v_cvt_pk_bf16_f32 v27, v86, v87\n"
  "s_waitcnt lgkmcnt(2)\n"
  "v_mfma_f32_32x32x16_bf16 v[96:111], v[64:67], v[48:51], v[96:111]\n"
  "ds_read_b128 v[64:67], v2 offset:32768\n"
  "v_mfma_f32_32x32x16_bf16 v[112:127], v[48:51], v[24:27], v[112:127]\n"
  "v_cvt_pk_bf16_f32 v28, v88, v89\n"
  "v_cvt_pk_bf16_f32 v29, v90, v91\n"
  "v_cvt_pk_bf16_f32 v30, v92, v93\n"
  "v_cvt_pk_bf16_f32 v31, v94, v95\n"
  "v_mfma_f32_32x32x16_bf16 v[96:111], v[68:71], v[52:55], v[96:111]\n"
  "ds_read_b128 v[68:71], v2 offset:33792\n"
  "v_mfma_f32_32x32x16_bf16 v[112:127], v[52:55], v[28:31], v[112:127]\n"
  "s_waitcnt lgkmcnt(2)\n"
  "v_mfma_f32_32x32x16_bf16 v[96:111], v[72:75], v[56:59], v[96:111]\n"
  "ds_read_b128 v[72:75], v2 offset:34816\n"
  "v_mfma_f32_32x32x16_bf16 v[96:111], v[76:79], v[60:63], v[96:111]\n"
  "ds_read_b128 v[76:79], v2 offset:35840\n"
  "s_cmp_eq_u32 s50, 1\n"
  "s_cbranch_scc1 .Lat_a%=\n"
  "s_waitcnt lgkmcnt(2)\n"
  "v_mfma_f32_32x32x16_bf16 v[80:95], v[32:35], v[64:67], 0\n"
  "ds_read_b128 v[64:67], v2 offset:36864\n"
  "v_mfma_f32_32x32x16_bf16 v[80:95], v[36:39], v[68:71], v[80:95]\n"
  "ds_read_b128 v[68:71], v2 offset:37888\n"
  "s_waitcnt lgkmcnt(2)\n"
  "v_mfma_f32_32x32x16_bf16 v[80:95], v[40:43], v[72:75], v[80:95]\n"
  "ds_read_b128 v[72:75], v2 offset:38912\n"
  "s_nop 0\n"
  "v_cvt_pk_bf16_f32 v24, v96, v97\n"
  "v_cvt_pk_bf16_f32 v25, v98, v99\n"
  "v_cvt_pk_bf16_f32 v26, v100, v101\n"
  "v_cvt_pk_bf16_f32 v27, v102, v103\n"
  "v_mfma_f32_32x32x16_bf16 v[80:95], v[44:47], v[76:79], v[80:95]\n"
  "ds_read_b128 v[76:79], v2 offset:39936\n"
  "v_mfma_f32_32x32x16_bf16 v[112:127], v[56:59], v[24:27], v[112:127]\n"
  "v_cvt_pk_bf16_f32 v28, v104, v105\n"
  "v_cvt_pk_bf16_f32 v29, v106, v107\n"
  "v_cvt_pk_bf16_f32 v30, v108, v109\n"
  "v_cvt_pk_bf16_f32 v31, v110, v111\n"
  "s_waitcnt lgkmcnt(2)\n"
  "v_mfma_f32_32x32x16_bf16 v[80:95], v[48:51], v[64:67], v[80:95]\n"
  "ds_read_b128 v[64:67], v2 offset:40960\n"
  "v_mfma_f32_32x32x16_bf16 v[112:127], v[60:63], v[28:31], v[112:127]\n"
  "v_mfma_f32_32x32x16_bf16 v[80:95], v[52:55], v[68:71], v[80:95]\n"
  "ds_read_b128 v[68:71], v2 offset:41984\n"
  "s_waitcnt lgkmcnt(2)\n"
  "v_mfma_f32_32x32x16_bf16 v[80:95], v[56:59], v[72:75], v[80:95]\n"
  "ds_read_b128 v[72:75], v2 offset:43008\n"
  "v_mfma_f32_32x32x16_bf16 v[80:95], v[60:63], v[76:79], v[80:95]\n"
  "ds_read_b128 v[76:79], v2 offset:44032\n"
  "s_waitcnt lgkmcnt(2)\n"
  "v_mfma_f32_32x32x16_bf16 v[96:111], v[32:35], v[64:67], 0\n"
  "ds_read_b128 v[64:67], v2 offset:45056\n"
  "s_nop 1\n"
  "v_bfi_b32 v16, v9, v113, v112\n"
  "v_bfi_b32 v17, v9, v115, v114\n"
  "v_bfi_b32 v12, v10, v17, v16\n"
  "v_bfi_b32 v16, v9, v117, v116\n"
  "v_bfi_b32 v17, v9, v119, v118\n"
  "v_mfma_f32_32x32x16_bf16 v[96:111], v[36:39], v[68:71], v[96:111]\n"
  "ds_read_b128 v[68:71], v2 offset:46080\n"
  "v_bfi_b32 v13, v10, v17, v16\n"
  "v_bfi_b32 v16, v9, v121, v120\n"
  "v_bfi_b32 v17, v9, v123, v122\n"
  "v_bfi_b32 v14, v10, v17, v16\n"
  "v_mul_f32 v12, v12, v0\n"
  "s_waitcnt lgkmcnt(2)\n"
  "v_mfma_f32_32x32x16_bf16 v[96:111], v[40:43], v[72:75], v[96:111]\n"
  "ds_read_b128 v[72:75], v2 offset:47104\n"
  "v_bfi_b32 v16, v9, v125, v124\n"
  "v_bfi_b32 v17, v9, v127, v126\n"
  "v_bfi_b32 v15, v10, v17, v16\n"
  "ds_read_b128 v[112:115], v4 offset:128\n"
  "ds_read_b128 v[116:119], v4 offset:256\n"
  "v_mfma_f32_32x32x16_bf16 v[96:111], v[44:47], v[76:79], v[96:111]\n"
  "ds_read_b128 v[76:79], v2 offset:48128\n"
  "v_cvt_pk_bf16_f32 v24, v80, v81\n"
  "v_cvt_pk_bf16_f32 v25, v82, v83\n"
  "v_cvt_pk_bf16_f32 v26, v84, v85\n"
  "v_cvt_pk_bf16_f32 v27, v86, v87\n"
  "v_mul_f32 v13, v13, v0\n"
  "s_waitcnt lgkmcnt(4)\n"
  "v_mfma_f32_32x32x16_bf16 v[96:111], v[48:51], v[64:67], v[96:111]\n"
  "ds_read_b128 v[64:67], v2 offset:49152\n"
  "v_cvt_pk_bf16_f32 v28, v88, v89\n"
  "v_cvt_pk_bf16_f32 v29, v90, v91\n"
  "v_cvt_pk_bf16_f32 v30, v92, v93\n"
  "v_cvt_pk_bf16_f32 v31, v94, v95\n"
  "v_mul_f32 v14, v14, v0\n"
  "v_mfma_f32_32x32x16_bf16 v[96:111], v[52:55], v[68:71], v[96:111]\n"
  "ds_read_b128 v[68:71], v2 offset:50176\n"
  "v_mul_f32 v15, v15, v0\n"
  "v_mul_f32 v12, v12, v20\n"
  "v_mul_f32 v13, v13, v21\n"
  "v_mul_f32 v14, v14, v22\n"
  "v_mul_f32 v15, v15, v23\n"
  "s_waitcnt lgkmcnt(2)\n"
  "v_mfma_f32_32x32x16_bf16 v[96:111], v[56:59], v[72:75], v[96:111]\n"
  "ds_read_b128 v[72:75], v2 offset:51200\n"
  "v_max3_f32 v16, v12, v13, v14\n"
  "v_max_f32 v16, v16, v15\n"
  "v_mov_b32 v17, v16\n"
  "v_mfma_f32_32x32x16_bf16 v[96:111], v[60:63], v[76:79], v[96:111]\n"
  "ds_read_b128 v[76:79], v2 offset:52224\n"
  "v_permlane32_swap_b32 v16, v17\n"
  "v_max_f32 v16, v16, v17\n"
  "v_sub_f32 v12, v12, v16\n"
  "v_sub_f32 v13, v13, v16\n"
  "v_sub_f32 v14, v14, v16\n"
  "s_waitcnt lgkmcnt(2)\n"
  "v_mfma_f32_32x32x16_bf16 v[80:95], v[32:35], v[64:67], 0\n"
  "ds_read_b128 v[64:67], v2 offset:53248\n"
  "v_sub_f32 v15, v15, v16\n"
  "v_exp_f32 v12, v12\n"
  "v_exp_f32 v13, v13\n"
  "v_mfma_f32_32x32x16_bf16 v[80:95], v[36:39], v[68:71], v[80:95]\n"
  "ds_read_b128 v[68:71], v2 offset:54272\n"
  "v_exp_f32 v14, v14\n"
  "v_exp_f32 v15, v15\n"
  "v_add_f32 v16, v12, v13\n"
  "s_waitcnt lgkmcnt(2)\n"
  "v_mfma_f32_32x32x16_bf16 v[80:95], v[40:43], v[72:75], v[80:95]\n"
  "ds_read_b128 v[72:75], v2 offset:55296\n"
  "v_add_f32 v17, v14, v15\n"
  "v_add_f32 v16, v16, v17\n"
  "v_mov_b32 v17, v16\n"
  "v_mul_f32 v12, v12, v20\n"
  "v_mul_f32 v13, v13, v21\n"
  "v_mfma_f32_32x32x16_bf16 v[80:95], v[44:47], v[76:79], v[80:95]\n"
  "ds_read_b128 v[76:79], v2 offset:56320\n"
  "v_permlane32_swap_b32 v16, v17\n"
  "v_add_f32 v16, v16, v17\n"
  "v_rcp_f32 v16, v16\n"
  "v_mul_f32 v14, v14, v22\n"
  "s_waitcnt lgkmcnt(2)\n"
  "v_mfma_f32_32x32x16_bf16 v[80:95], v[48:51], v[64:67], v[80:95]\n"
  "ds_read_b128 v[64:67], v2 offset:57344\n"
  "v_mul_f32 v15, v15, v23\n"
  "v_mul_f32 v16, v16, v5\n"
  "v_mul_f32 v12, v12, v16\n"
  "v_mul_f32 v13, v13, v16\n"
  "v_mul_f32 v14, v14, v16\n"
  "v_mfma_f32_32x32x16_bf16 v[80:95], v[52:55], v[68:71], v[80:95]\n"
  "ds_read_b128 v[68:71], v2 offset:58368\n"
  "v_mul_f32 v15, v15, v16\n"
  "v_mul_f32 v16, v12, v112\n"
  "v_mul_f32 v17, v12, v116\n"
  "v_fmac_f32 v16, v13, v113\n"
  "v_fmac_f32 v17, v13, v117\n"
  "s_waitcnt lgkmcnt(2)\n"
  "v_mfma_f32_32x32x16_bf16 v[80:95], v[56:59], v[72:75], v[80:95]\n"
  "ds_read_b128 v[72:75], v2 offset:59392\n"
  "v_fmac_f32 v16, v14, v114\n"
  "v_fmac_f32 v17, v14, v118\n"
  "v_fmac_f32 v16, v15, v115\n"
  "v_fmac_f32 v17, v15, v119\n"
  "v_cvt_pk_bf16_f32 v112, v96, v97\n"
  "v_mfma_f32_32x32x16_bf16 v[80:95], v[60:63], v[76:79], v[80:95]\n"
  "ds_read_b128 v[76:79], v2 offset:60416\n"
  "v_cvt_pk_bf16_f32 v113, v98, v99\n"
  "v_cvt_pk_bf16_f32 v114, v100, v101\n"
  "v_cvt_pk_bf16_f32 v115, v102, v103\n"
  "v_cvt_pk_bf16_f32 v116, v104, v105\n"
  "v_cvt_pk_bf16_f32 v117, v106, v107\n"
  "v_cvt_pk_bf16_f32 v118, v108, v109\n"
  "v_cvt_pk_bf16_f32 v119, v110, v111\n"
  "s_waitcnt lgkmcnt(2)\n"
  "v_mfma_f32_32x32x16_bf16 v[96:111], v[32:35], v[64:67], 0\n"
  "ds_read_b128 v[64:67], v2 offset:61440\n"
  "v_permlane32_swap_b32 v16, v17\n"
  "v_add_f32 v16, v16, v17\n"
  "v_mov_b32 v17, v16\n"
  "v_cvt_pk_bf16_f32 v12, v12, v12\n"
  "v_cvt_pk_bf16_f32 v13, v13, v13\n"
  "v_mfma_f32_32x32x16_bf16 v[96:111], v[36:39], v[68:71], v[96:111]\n"
  "ds_read_b128 v[68:71], v2 offset:62464\n"
  "v_permlane32_swap_b32 v16, v17\n"
  "v_add_f32 v1, v1, v16\n"
  "v_add_f32 v7, v7, v17\n"
  "v_cvt_pk_bf16_f32 v14, v14, v14\n"
  "v_cvt_pk_bf16_f32 v15, v15, v15\n"
  "s_waitcnt lgkmcnt(2)\n"
  "v_mfma_f32_32x32x16_bf16 v[96:111], v[40:43], v[72:75], v[96:111]\n"
  "ds_read_b128 v[72:75], v2 offset:63488\n"
  "v_xor_b32 v9, 0xffff, v9\n"
  "v_and_b32 v3, v9, v10\n"
  "v_xor_b32 v9, v9, v3\n"
  "v_and_b32 v0, 0x80000000, v5\n"
  "v_or_b32 v0, 0x3fb8aa3b, v0\n"
  "v_mul_f32 v7, v7, v0\n"
  "v_mfma_f32_32x32x16_bf16 v[96:111], v[44:47], v[76:79], v[96:111]\n"
  "ds_read_b128 v[76:79], v2 offset:64512\n"
  "v_cvt_pk_bf16_f32 v120, v80, v81\n"
  "v_cvt_pk_bf16_f32 v121, v82, v83\n"
  "v_cvt_pk_bf16_f32 v122, v84, v85\n"
  "v_cvt_pk_bf16_f32 v123, v86, v87\n"
  "v_and_b32 v16, v12, v9\n"
  "s_waitcnt lgkmcnt(2)\n"
  "v_mfma_f32_32x32x16_bf16 v[96:111], v[48:51], v[64:67], v[96:111]\n"
  "v_cvt_pk_bf16_f32 v124, v88, v89\n"
  "v_cvt_pk_bf16_f32 v125, v90, v91\n"
  "v_cvt_pk_bf16_f32 v126, v92, v93\n"
  "v_cvt_pk_bf16_f32 v127, v94, v95\n"
  "v_and_b32 v17, v12, v3\n"
  "v_mfma_f32_32x32x16_bf16 v[96:111], v[52:55], v[68:71], v[96:111]\n"
  "v_and_b32 v18, v13, v9\n"
  "v_and_b32 v19, v13, v3\n"
  "v_and_b32 v20, v14, v9\n"
  "v_and_b32 v21, v14, v3\n"
  "v_and_b32 v22, v15, v9\n"
  "s_waitcnt lgkmcnt(0)\n"
  "v_mfma_f32_32x32x16_bf16 v[96:111], v[56:59], v[72:75], v[96:111]\n"
  "v_and_b32 v23, v15, v3\n"
  "v_lshlrev_b32 v80, 16, v32\n"
  "v_and_b32 v81, 0xffff0000, v32\n"
  "v_lshlrev_b32 v82, 16, v33\n"
  "v_and_b32 v83, 0xffff0000, v33\n"
  "v_mfma_f32_32x32x16_bf16 v[96:111], v[60:63], v[76:79], v[96:111]\n"
  "v_lshlrev_b32 v84, 16, v34\n"
  "v_and_b32 v85, 0xffff0000, v34\n"
  "v_lshlrev_b32 v86, 16, v35\n"
  "v_and_b32 v87, 0xffff0000, v35\n"
  "v_lshlrev_b32 v88, 16, v36\n"
  "v_and_b32 v89, 0xffff0000, v36\n"
  "v_lshlrev_b32 v90, 16, v37\n"
  "v_and_b32 v91, 0xffff0000, v37\n"
  "v_lshlrev_b32 v92, 16, v38\n"
  "v_and_b32 v93, 0xffff0000, v38\n"
  "v_lshlrev_b32 v94, 16, v39\n"
  "v_and_b32 v95, 0xffff0000, v39\n"
  ".Lat_t%=:\n"
  "s_nop 1\n"
  "v_mfma_f32_32x32x16_bf16 v[80:95], v[24:27], v[16:19], v[80:95]\n"
  "v_mfma_f32_32x32x16_bf16 v[80:95], v[28:31], v[20:23], v[80:95]\n"
  "v_lshlrev_b32 v64, 16, v40\n"
  "v_and_b32 v65, 0xffff0000, v40\n"
  "v_lshlrev_b32 v66, 16, v41\n"
  "v_and_b32 v67, 0xffff0000, v41\n"
  "v_lshlrev_b32 v68, 16, v42\n"
  "v_and_b32 v69, 0xffff0000, v42\n"
  "v_lshlrev_b32 v70, 16, v43\n"
  "v_and_b32 v71, 0xffff0000, v43\n"
  "v_lshlrev_b32 v72, 16, v44\n"
  "v_and_b32 v73, 0xffff0000, v44\n"
  "v_lshlrev_b32 v74, 16, v45\n"
  "v_and_b32 v75, 0xffff0000, v45\n"
  "v_lshlrev_b32 v76, 16, v46\n"
  "v_and_b32 v77, 0xffff0000, v46\n"
  "v_lshlrev_b32 v78, 16, v47\n"
  "v_and_b32 v79, 0xffff0000, v47\n"
  "s_nop 1\n"
  "v_mfma_f32_32x32x16_bf16 v[64:79], v[112:115], v[16:19], v[64:79]\n"
  "v_mfma_f32_32x32x16_bf16 v[64:79], v[116:119], v[20:23], v[64:79]\n"
  "v_cvt_pk_bf16_f32 v32, v96, v97\n"
  "v_cvt_pk_bf16_f32 v33, v98, v99\n"
  "v_cvt_pk_bf16_f32 v34, v100, v101\n"
  "v_cvt_pk_bf16_f32 v35, v102, v103\n"
  "v_cvt_pk_bf16_f32 v36, v104, v105\n"
  "v_cvt_pk_bf16_f32 v37, v106, v107\n"
  "v_cvt_pk_bf16_f32 v38, v108, v109\n"
  "v_cvt_pk_bf16_f32 v39, v110, v111\n"
  "v_lshlrev_b32 v96, 16, v48\n"
  "v_and_b32 v97, 0xffff0000, v48\n"
  "v_lshlrev_b32 v98, 16, v49\n"
  "v_and_b32 v99, 0xffff0000, v49\n"
  "v_lshlrev_b32 v100, 16, v50\n"
  "v_and_b32 v101, 0xffff0000, v50\n"
  "v_lshlrev_b32 v102, 16, v51\n"
  "v_and_b32 v103, 0xffff0000, v51\n"
  "v_lshlrev_b32 v104, 16, v52\n"
  "v_and_b32 v105, 0xffff0000, v52\n"
  "v_lshlrev_b32 v106, 16, v53\n"
  "v_and_b32 v107, 0xffff0000, v53\n"
  "v_lshlrev_b32 v108, 16, v54\n"
  "v_and_b32 v109, 0xffff0000, v54\n"
  "v_lshlrev_b32 v110, 16, v55\n"
  "v_and_b32 v111, 0xffff0000, v55\n"
  "s_nop 1\n"
  "v_mfma_f32_32x32x16_bf16 v[96:111], v[120:123], v[16:19], v[96:111]\n"
  "v_mfma_f32_32x32x16_bf16 v[96:111], v[124:127], v[20:23], v[96:111]\n"
  "v_lshlrev_b32 v40, 16, v56\n"
  "v_and_b32 v41, 0xffff0000, v56\n"
  "v_lshlrev_b32 v42, 16, v57\n"
  "v_and_b32 v43, 0xffff0000, v57\n"
  "v_lshlrev_b32 v44, 16, v58\n"
  "v_and_b32 v45, 0xffff0000, v58\n"
  "v_lshlrev_b32 v46, 16, v59\n"
  "v_and_b32 v47, 0xffff0000, v59\n"
  "v_lshlrev_b32 v48, 16, v60\n"
  "v_and_b32 v49, 0xffff0000, v60\n"
  "v_lshlrev_b32 v50, 16, v61\n"
  "v_and_b32 v51, 0xffff0000, v61\n"
  "v_lshlrev_b32 v52, 16, v62\n"
  "v_and_b32 v53, 0xffff0000, v62\n"
  "v_lshlrev_b32 v54, 16, v63\n"
  "v_and_b32 v55, 0xffff0000, v63\n"
  "s_nop 1\n"
  "v_mfma_f32_32x32x16_bf16 v[40:55], v[32:35], v[16:19], v[40:55]\n"
  "v_mfma_f32_32x32x16_bf16 v[40:55], v[36:39], v[20:23], v[40:55]\n"
  "s_setprio 0\n"
  "v_mul_f32 v12, v80, v80\n"
  "v_mul_f32 v13, v81, v81\n"
  "v_fmac_f32 v12, v82, v82\n"
  "v_fmac_f32 v13, v83, v83\n"
  "v_fmac_f32 v12, v84, v84\n"
  "v_fmac_f32 v13, v85, v85\n"
  "v_fmac_f32 v12, v86, v86\n"
  "v_fmac_f32 v13, v87, v87\n"
  "v_fmac_f32 v12, v88, v88\n"
  "v_fmac_f32 v13, v89, v89\n"
  "v_fmac_f32 v12, v90, v90\n"
  "v_fmac_f32 v13, v91, v91\n"
  "v_fmac_f32 v12, v92, v92\n"
  "v_fmac_f32 v13, v93, v93\n"
  "v_fmac_f32 v12, v94, v94\n"
  "v_fmac_f32 v13, v95, v95\n"
  "v_fmac_f32 v12, v64, v64\n"
  "v_fmac_f32 v13, v65, v65\n"
  "v_fmac_f32 v12, v66, v66\n"
  "v_fmac_f32 v13, v67, v67\n"
  "v_fmac_f32 v12, v68, v68\n"
  "v_fmac_f32 v13, v69, v69\n"
  "v_fmac_f32 v12, v70, v70\n"
  "v_fmac_f32 v13, v71, v71\n"
  "v_fmac_f32 v12, v72, v72\n"
  "v_fmac_f32 v13, v73, v73\n"
  "v_fmac_f32 v12, v74, v74\n"
  "v_fmac_f32 v13, v75, v75\n"
  "v_fmac_f32 v12, v76, v76\n"
  "v_fmac_f32 v13, v77, v77\n"
  "v_fmac_f32 v12, v78, v78\n"
  "v_fmac_f32 v13, v79, v79\n"
  "v_fmac_f32 v12, v96, v96\n"
  "v_fmac_f32 v13, v97, v97\n"
  "v_fmac_f32 v12, v98, v98\n"
  "v_fmac_f32 v13, v99, v99\n"
  "v_fmac_f32 v12, v100, v100\n"
  "v_fmac_f32 v13, v101, v101\n"
  "v_fmac_f32 v12, v102, v102\n"
  "v_fmac_f32 v13, v103, v103\n"
  "v_fmac_f32 v12, v104, v104\n"
  "v_fmac_f32 v13, v105, v105\n"
  "v_fmac_f32 v12, v106, v106\n"
  "v_fmac_f32 v13, v107, v107\n"
  "v_fmac_f32 v12, v108, v108\n"
  "v_fmac_f32 v13, v109, v109\n"
  "v_fmac_f32 v12, v110, v110\n"
  "v_fmac_f32 v13, v111, v111\n"
  "v_fmac_f32 v12, v40, v40\n"
  "v_fmac_f32 v13, v41, v41\n"
  "v_fmac_f32 v12, v42, v42\n"
  "v_fmac_f32 v13, v43, v43\n"
  "v_fmac_f32 v12, v44, v44\n"
  "v_fmac_f32 v13, v45, v45\n"
  "v_fmac_f32 v12, v46, v46\n"
  "v_fmac_f32 v13, v47, v47\n"
  "v_fmac_f32 v12, v48, v48\n"
  "v_fmac_f32 v13, v49, v49\n"
  "v_fmac_f32 v12, v50, v50\n"
  "v_fmac_f32 v13, v51, v51\n"
  "v_fmac_f32 v12, v52, v52\n"
  "v_fmac_f32 v13, v53, v53\n"
  "v_fmac_f32 v12, v54, v54\n"
  "v_fmac_f32 v13, v55, v55\n"
  "v_add_f32 v12, v12, v13\n"
  "v_mul_f32 v14, 0x3c000000, v1\n"
  "v_mov_b32 v13, v12\n"
  "v_permlane16_swap_b32 v80, v64\n"
  "v_mul_f32 v14, v14, v14\n"
  "v_permlane32_swap_b32 v12, v13\n"
  "v_add_f32 v12, v12, v13\n"
  "v_fma_f32 v12, v12, s56, -v14\n"
  "v_max_f32 v12, 0, v12\n"
  "v_add_f32 v12, v12, v8\n"
  "v_rsq_f32 v12, v12\n"
  "v_permlane16_swap_b32 v81, v65\n"
  "v_mul_f32 v12, v12, v7\n"
  "v_permlane16_swap_b32 v82, v66\n"
  "v_permlane16_swap_b32 v83, v67\n"
  "v_max_f32_dpp v13, v12, v12 row_ror:4 row_mask:0xf bank_mask:0xf\n"
  "v_permlane16_swap_b32 v84, v68\n"
  "v_permlane16_swap_b32 v85, v69\n"
  "v_max_f32_dpp v13, v13, v13 row_ror:8 row_mask:0xf bank_mask:0xf\n"
  "v_mov_b32 v14, v13\n"
  "v_permlane16_swap_b32 v86, v70\n"
  "v_permlane16_swap_b32 v87, v71\n"
  "v_permlane16_swap_b32 v13, v14\n"
  "v_max_f32 v13, v13, v14\n"
  "v_sub_f32 v12, v12, v13\n"
  "v_exp_f32 v12, v12\n"
  "v_permlane16_swap_b32 v88, v72\n"
  "v_permlane16_swap_b32 v89, v73\n"
  "v_add_f32_dpp v13, v12, v12 row_ror:4 row_mask:0xf bank_mask:0xf\n"
  "v_mul_f32 v12, v12, v6\n"
  "v_permlane16_swap_b32 v90, v74\n"
  "v_add_f32_dpp v13, v13, v13 row_ror:8 row_mask:0xf bank_mask:0xf\n"
  "v_mov_b32 v14, v13\n"
  "v_permlane16_swap_b32 v91, v75\n"
  "v_permlane16_swap_b32 v92, v76\n"
  "v_permlane16_swap_b32 v13, v14\n"
  "v_add_f32 v13, v13, v14\n"
  "v_rcp_f32 v13, v13\n"
  "v_permlane16_swap_b32 v93, v77\n"
  "v_mul_f32 v12, v12, v13\n"
  "v_mov_b32 v13, v12\n"
  "v_permlane16_swap_b32 v94, v78\n"
  "v_permlane16_swap_b32 v95, v79\n"
  "v_permlane16_swap_b32 v12, v13\n"
  "v_permlane16_swap_b32 v96, v40\n"
  "v_permlane16_swap_b32 v97, v41\n"
  "v_permlane16_swap_b32 v98, v42\n"
  "v_permlane16_swap_b32 v99, v43\n"
  "v_permlane16_swap_b32 v100, v44\n"
  "v_permlane16_swap_b32 v101, v45\n"
  "v_permlane16_swap_b32 v102, v46\n"
  "v_permlane16_swap_b32 v103, v47\n"
  "v_permlane16_swap_b32 v104, v48\n"
  "v_permlane16_swap_b32 v105, v49\n"
  "v_permlane16_swap_b32 v106, v50\n"
  "v_permlane16_swap_b32 v107, v51\n"
  "v_permlane16_swap_b32 v108, v52\n"
  "v_permlane16_swap_b32 v109, v53\n"
  "v_permlane16_swap_b32 v110, v54\n"
  "v_permlane16_swap_b32 v111, v55\n"
  "v_mul_f32 v80, v80, v12\n"
  "v_mul_f32 v81, v81, v12\n"
  "v_mul_f32 v82, v82, v12\n"
  "v_mul_f32 v83, v83, v12\n"
  "v_mul_f32 v84, v84, v12\n"
  "v_mul_f32 v85, v85, v12\n"
  "v_mul_f32 v86, v86, v12\n"
  "v_mul_f32 v87, v87, v12\n"
  "v_mul_f32 v88, v88, v12\n"
  "v_mul_f32 v89, v89, v12\n"
  "v_mul_f32 v90, v90, v12\n"
  "v_mul_f32 v91, v91, v12\n"
  "v_mul_f32 v92, v92, v12\n"
  "v_mul_f32 v93, v93, v12\n"
  "v_mul_f32 v94, v94, v12\n"
  "v_mul_f32 v95, v95, v12\n"
  "v_fmac_f32 v80, v64, v13\n"
  "v_fmac_f32 v81, v65, v13\n"
  "v_fmac_f32 v82, v66, v13\n"
  "v_fmac_f32 v83, v67, v13\n"
  "v_fmac_f32 v84, v68, v13\n"
  "v_fmac_f32 v85, v69, v13\n"
  "v_fmac_f32 v86, v70, v13\n"
  "v_fmac_f32 v87, v71, v13\n"
  "v_fmac_f32 v88, v72, v13\n"
  "v_fmac_f32 v89, v73, v13\n"
  "v_fmac_f32 v90, v74, v13\n"
  "v_fmac_f32 v91, v75, v13\n"
  "v_fmac_f32 v92, v76, v13\n"
  "v_fmac_f32 v93, v77, v13\n"
  "v_fmac_f32 v94, v78, v13\n"
  "v_fmac_f32 v95, v79, v13\n"
  "v_add_f32_dpp v80, v80, v80 row_ror:8 row_mask:0xf bank_mask:0x3\n"
  "v_add_f32_dpp v80, v88, v88 row_ror:8 row_mask:0xf bank_mask:0xc\n"
  "v_add_f32_dpp v81, v81, v81 row_ror:8 row_mask:0xf bank_mask:0x3\n"
  "v_add_f32_dpp v81, v89, v89 row_ror:8 row_mask:0xf bank_mask:0xc\n"
  "v_add_f32_dpp v82, v82, v82 row_ror:8 row_mask:0xf bank_mask:0x3\n"
  "v_add_f32_dpp v82, v90, v90 row_ror:8 row_mask:0xf bank_mask:0xc\n"
  "v_add_f32_dpp v83, v83, v83 row_ror:8 row_mask:0xf bank_mask:0x3\n"
  "v_add_f32_dpp v83, v91, v91 row_ror:8 row_mask:0xf bank_mask:0xc\n"
  "v_add_f32_dpp v84, v84, v84 row_ror:8 row_mask:0xf bank_mask:0x3\n"
  "v_add_f32_dpp v84, v92, v92 row_ror:8 row_mask:0xf bank_mask:0xc\n"
  "v_add_f32_dpp v85, v85, v85 row_ror:8 row_mask:0xf bank_mask:0x3\n"
  "v_add_f32_dpp v85, v93, v93 row_ror:8 row_mask:0xf bank_mask:0xc\n"
  "v_add_f32_dpp v86, v86, v86 row_ror:8 row_mask:0xf bank_mask:0x3\n"
  "v_add_f32_dpp v86, v94, v94 row_ror:8 row_mask:0xf bank_mask:0xc\n"
  "v_add_f32_dpp v87, v87, v87 row_ror:8 row_mask:0xf bank_mask:0x3\n"
  "v_add_f32_dpp v87, v95, v95 row_ror:8 row_mask:0xf bank_mask:0xc\n"
  "v_add_f32_dpp v80, v80, v80 row_shl:4 row_mask:0xf bank_mask:0x5\n"
  "v_add_f32_dpp v80, v84, v84 row_shr:4 row_mask:0xf bank_mask:0xa\n"
  "v_add_f32_dpp v81, v81, v81 row_shl:4 row_mask:0xf bank_mask:0x5\n"
  "v_add_f32_dpp v81, v85, v85 row_shr:4 row_mask:0xf bank_mask:0xa\n"
  "v_add_f32_dpp v82, v82, v82 row_shl:4 row_mask:0xf bank_mask:0x5\n"
  "v_add_f32_dpp v82, v86, v86 row_shr:4 row_mask:0xf bank_mask:0xa\n"
  "v_add_f32_dpp v83, v83, v83 row_shl:4 row_mask:0xf bank_mask:0x5\n"
  "v_add_f32_dpp v83, v87, v87 row_shr:4 row_mask:0xf bank_mask:0xa\n"
  "v_cvt_pk_bf16_f32 v80, v80, v81\n"
  "v_cvt_pk_bf16_f32 v81, v82, v83\n"
  "global_store_dwordx2 v11, v[80:81], s[52:53] offset:0 sc0 sc1 nt\n"
  "v_mul_f32 v96, v96, v12\n"
  "v_mul_f32 v97, v97, v12\n"
  "v_mul_f32 v98, v98, v12\n"
  "v_mul_f32 v99, v99, v12\n"
  "v_mul_f32 v100, v100, v12\n"
  "v_mul_f32 v101, v101, v12\n"
  "v_mul_f32 v102, v102, v12\n"
  "v_mul_f32 v103, v103, v12\n"
  "v_mul_f32 v104, v104, v12\n"
  "v_mul_f32 v105, v105, v12\n"
  "v_mul_f32 v106, v106, v12\n"
  "v_mul_f32 v107, v107, v12\n"
  "v_mul_f32 v108, v108, v12\n"
  "v_mul_f32 v109, v109, v12\n"
  "v_mul_f32 v110, v110, v12\n"
  "v_mul_f32 v111, v111, v12\n"
  "v_fmac_f32 v96, v40, v13\n"
  "v_fmac_f32 v97, v41, v13\n"
  "v_fmac_f32 v98, v42, v13\n"
  "v_fmac_f32 v99, v43, v13\n"
  "v_fmac_f32 v100, v44, v13\n"
  "v_fmac_f32 v101, v45, v13\n"
  "v_fmac_f32 v102, v46, v13\n"
  "v_fmac_f32 v103, v47, v13\n"
  "v_fmac_f32 v104, v48, v13\n"
  "v_fmac_f32 v105, v49, v13\n"
  "v_fmac_f32 v106, v50, v13\n"
  "v_fmac_f32 v107, v51, v13\n"
  "v_fmac_f32 v108, v52, v13\n"
  "v_fmac_f32 v109, v53, v13\n"
  "v_fmac_f32 v110, v54, v13\n"
  "v_fmac_f32 v111, v55, v13\n"
  "v_add_f32_dpp v96, v96, v96 row_ror:8 row_mask:0xf bank_mask:0x3\n"
  "v_add_f32_dpp v96, v104, v104 row_ror:8 row_mask:0xf bank_mask:0xc\n"
  "v_add_f32_dpp v97, v97, v97 row_ror:8 row_mask:0xf bank_mask:0x3\n"
  "v_add_f32_dpp v97, v105, v105 row_ror:8 row_mask:0xf bank_mask:0xc\n"
  "v_add_f32_dpp v98, v98, v98 row_ror:8 row_mask:0xf bank_mask:0x3\n"
  "v_add_f32_dpp v98, v106, v106 row_ror:8 row_mask:0xf bank_mask:0xc\n"
  "v_add_f32_dpp v99, v99, v99 row_ror:8 row_mask:0xf bank_mask:0x3\n"
  "v_add_f32_dpp v99, v107, v107 row_ror:8 row_mask:0xf bank_mask:0xc\n"
  "v_add_f32_dpp v100, v100, v100 row_ror:8 row_mask:0xf bank_mask:0x3\n"
  "v_add_f32_dpp v100, v108, v108 row_ror:8 row_mask:0xf bank_mask:0xc\n"
  "v_add_f32_dpp v101, v101, v101 row_ror:8 row_mask:0xf bank_mask:0x3\n"
  "v_add_f32_dpp v101, v109, v109 row_ror:8 row_mask:0xf bank_mask:0xc\n"
  "v_add_f32_dpp v102, v102, v102 row_ror:8 row_mask:0xf bank_mask:0x3\n"
  "v_add_f32_dpp v102, v110, v110 row_ror:8 row_mask:0xf bank_mask:0xc\n"
  "v_add_f32_dpp v103, v103, v103 row_ror:8 row_mask:0xf bank_mask:0x3\n"
  "v_add_f32_dpp v103, v111, v111 row_ror:8 row_mask:0xf bank_mask:0xc\n"
  "v_add_f32_dpp v96, v96, v96 row_shl:4 row_mask:0xf bank_mask:0x5\n"
  "v_add_f32_dpp v96, v100, v100 row_shr:4 row_mask:0xf bank_mask:0xa\n"
  "v_add_f32_dpp v97, v97, v97 row_shl:4 row_mask:0xf bank_mask:0x5\n"
  "v_add_f32_dpp v97, v101, v101 row_shr:4 row_mask:0xf bank_mask:0xa\n"
  "v_add_f32_dpp v98, v98, v98 row_shl:4 row_mask:0xf bank_mask:0x5\n"
  "v_add_f32_dpp v98, v102, v102 row_shr:4 row_mask:0xf bank_mask:0xa\n"
  "v_add_f32_dpp v99, v99, v99 row_shl:4 row_mask:0xf bank_mask:0x5\n"
  "v_add_f32_dpp v99, v103, v103 row_shr:4 row_mask:0xf bank_mask:0xa\n"
  "v_cvt_pk_bf16_f32 v96, v96, v97\n"
  "v_cvt_pk_bf16_f32 v97, v98, v99\n"
  "global_store_dwordx2 v11, v[96:97], s[52:53] offset:128 sc0 sc1 nt\n"
  "s_branch .Lat_e%=\n"
  ".Lat_a%=:\n"
  "s_nop 8\n"
  "v_cvt_pk_bf16_f32 v24, v96, v97\n"
  "v_cvt_pk_bf16_f32 v25, v98, v99\n"
  "v_cvt_pk_bf16_f32 v26, v100, v101\n"
  "v_cvt_pk_bf16_f32 v27, v102, v103\n"
  "s_nop 1\n"
  "v_mfma_f32_32x32x16_bf16 v[112:127], v[56:59], v[24:27], v[112:127]\n"
  "v_cvt_pk_bf16_f32 v28, v104, v105\n"
  "v_cvt_pk_bf16_f32 v29, v106, v107\n"
  "v_cvt_pk_bf16_f32 v30, v108, v109\n"
  "v_cvt_pk_bf16_f32 v31, v110, v111\n"
  "s_nop 1\n"
  "v_mfma_f32_32x32x16_bf16 v[112:127], v[60:63], v[28:31], v[112:127]\n"
  "s_nop 11\n"
  "v_bfi_b32 v16, v9, v113, v112\n"
  "v_bfi_b32 v17, v9, v115, v114\n"
  "v_bfi_b32 v12, v10, v17, v16\n"
  "v_bfi_b32 v16, v9, v117, v116\n"
  "v_bfi_b32 v17, v9, v119, v118\n"
  "v_bfi_b32 v13, v10, v17, v16\n"
  "v_bfi_b32 v16, v9, v121, v120\n"
  "v_bfi_b32 v17, v9, v123, v122\n"
  "v_bfi_b32 v14, v10, v17, v16\n"
  "v_mul_f32 v12, v12, v0\n"
  "v_bfi_b32 v16, v9, v125, v124\n"
  "v_bfi_b32 v17, v9, v127, v126\n"
  "v_bfi_b32 v15, v10, v17, v16\n"
  "ds_read_b128 v[112:115], v4 offset:128\n"
  "ds_read_b128 v[116:119], v4 offset:256\n"
  "v_mul_f32 v13, v13, v0\n"
  "v_mul_f32 v14, v14, v0\n"
  "v_mul_f32 v15, v15, v0\n"
  "v_mul_f32 v12, v12, v20\n"
  "v_mul_f32 v13, v13, v21\n"
  "v_mul_f32 v14, v14, v22\n"
  "v_mul_f32 v15, v15, v23\n"
  "v_max3_f32 v16, v12, v13, v14\n"
  "v_max_f32 v16, v16, v15\n"
  "v_mov_b32 v17, v16\n"
  "s_nop 1\n"
  "v_permlane32_swap_b32 v16, v17\n"
  "v_max_f32 v16, v16, v17\n"
  "v_sub_f32 v12, v12, v16\n"
  "v_sub_f32 v13, v13, v16\n"
  "v_sub_f32 v14, v14, v16\n"
  "v_sub_f32 v15, v15, v16\n"
  "v_exp_f32 v12, v12\n"
  "v_exp_f32 v13, v13\n"
  "v_exp_f32 v14, v14\n"
  "v_exp_f32 v15, v15\n"
  "v_add_f32 v16, v12, v13\n"
  "v_add_f32 v17, v14, v15\n"
  "v_add_f32 v16, v16, v17\n"
  "v_mov_b32 v17, v16\n"
  "v_mul_f32 v12, v12, v20\n"
  "v_mul_f32 v13, v13, v21\n"
  "v_permlane32_swap_b32 v16, v17\n"
  "v_add_f32 v16, v16, v17\n"
  "v_rcp_f32 v16, v16\n"
  "v_mul_f32 v14, v14, v22\n"
  "v_mul_f32 v15, v15, v23\n"
  "v_mul_f32 v16, v16, v5\n"
  "v_mul_f32 v12, v12, v16\n"
  "v_mul_f32 v13, v13, v16\n"
  "v_mul_f32 v14, v14, v16\n"
  "v_mul_f32 v15, v15, v16\n"
  "s_waitcnt lgkmcnt(1)\n"
  "v_mul_f32 v16, v12, v112\n"
  "s_waitcnt lgkmcnt(0)\n"
  "v_mul_f32 v17, v12, v116\n"
  "v_fmac_f32 v16, v13, v113\n"
  "v_fmac_f32 v17, v13, v117\n"
  "v_fmac_f32 v16, v14, v114\n"
  "v_fmac_f32 v17, v14, v118\n"
  "v_fmac_f32 v16, v15, v115\n"
  "v_fmac_f32 v17, v15, v119\n"
  "s_nop 1\n"
  "v_permlane32_swap_b32 v16, v17\n"
  "v_add_f32 v16, v16, v17\n"
  "v_mov_b32 v17, v16\n"
  "v_cvt_pk_bf16_f32 v12, v12, v12\n"
  "v_cvt_pk_bf16_f32 v13, v13, v13\n"
  "v_permlane32_swap_b32 v16, v17\n"
  "v_add_f32 v1, v1, v16\n"
  "v_add_f32 v7, v7, v17\n"
  "v_cvt_pk_bf16_f32 v14, v14, v14\n"
  "v_cvt_pk_bf16_f32 v15, v15, v15\n"
  "v_and_b32 v0, 0x80000000, v5\n"
  "v_or_b32 v0, 0x3fb8aa3b, v0\n"
  "v_mul_f32 v7, v7, v0\n"
  "v_add_u32 v16, s51, v2\n"
  "v_mov_b32 v20, v1\n"
  "v_mov_b32 v21, v7\n"
  "v_mov_b32 v22, 1\n"
  "ds_write_b128 v16, v[12:15]\n"
  "ds_write_b64 v16, v[20:21] offset:1024\n"
  "ds_write_b32 v16, v22 offset:1032\n"
  "s_waitcnt lgkmcnt(0)\n"
  "s_branch .Lat_e%=\n"
  ".Lat_b%=:\n"
  "v_add_u32 v4, s51, v2\n"
  "s_waitcnt lgkmcnt(7)\n"
  "v_mfma_f32_32x32x16_bf16 v[80:95], v[32:35], v[64:67], 0\n"
  "ds_read_b128 v[64:67], v2 offset:4096\n"
  "s_waitcnt lgkmcnt(7)\n"
  "v_mfma_f32_32x32x16_bf16 v[80:95], v[36:39], v[68:71], v[80:95]\n"
  "ds_read_b128 v[68:71], v2 offset:5120\n"
  "s_waitcnt lgkmcnt(7)\n"
  "v_mfma_f32_32x32x16_bf16 v[80:95], v[40:43], v[72:75], v[80:95]\n"
  "ds_read_b128 v[72:75], v2 offset:6144\n"
  "s_waitcnt lgkmcnt(7)\n"
  "v_mfma_f32_32x32x16_bf16 v[80:95], v[44:47], v[76:79], v[80:95]\n"
  "ds_read_b128 v[76:79], v2 offset:7168\n"
  "s_waitcnt lgkmcnt(2)\n"
  "v_mfma_f32_32x32x16_bf16 v[80:95], v[48:51], v[64:67], v[80:95]\n"
  "ds_read_b128 v[64:67], v2 offset:8192\n"
  "v_mfma_f32_32x32x16_bf16 v[80:95], v[52:55], v[68:71], v[80:95]\n"
  "ds_read_b128 v[68:71], v2 offset:9216\n"
  "s_waitcnt lgkmcnt(2)\n"
  "v_mfma_f32_32x32x16_bf16 v[80:95], v[56:59], v[72:75], v[80:95]\n"
  "ds_read_b128 v[72:75], v2 offset:10240\n"
  "v_mfma_f32_32x32x16_bf16 v[80:95], v[60:63], v[76:79], v[80:95]\n"
  "ds_read_b128 v[76:79], v2 offset:11264\n"
  "s_waitcnt lgkmcnt(2)\n"
  "v_mfma_f32_32x32x16_bf16 v[96:111], v[32:35], v[64:67], 0\n"
  "ds_read_b128 v[64:67], v2 offset:12288\n"
  "v_mfma_f32_32x32x16_bf16 v[96:111], v[36:39], v[68:71], v[96:111]\n"
  "ds_read_b128 v[68:71], v2 offset:13312\n"
  "s_waitcnt lgkmcnt(2)\n"
  "v_mfma_f32_32x32x16_bf16 v[96:111], v[40:43], v[72:75], v[96:111]\n"
  "ds_read_b128 v[72:75], v2 offset:14336\n"
  "v_mfma_f32_32x32x16_bf16 v[96:111], v[44:47], v[76:79], v[96:111]\n"
  "ds_read_b128 v[76:79], v2 offset:15360\n"
  "s_nop 0\n"
  "v_cvt_pk_bf16_f32 v24, v80, v81\n"
  "v_cvt_pk_bf16_f32 v25, v82, v83\n"
  "v_cvt_pk_bf16_f32 v26, v84, v85\n"
  "v_cvt_pk_bf16_f32 v27, v86, v87\n"
  "s_waitcnt lgkmcnt(2)\n"
  "v_mfma_f32_32x32x16_bf16 v[96:111], v[48:51], v[64:67], v[96:111]\n"
  "ds_read_b128 v[64:67], v2 offset:16384\n"
  "v_cvt_pk_bf16_f32 v28, v88, v89\n"
  "v_cvt_pk_bf16_f32 v29, v90, v91\n"
  "v_cvt_pk_bf16_f32 v30, v92, v93\n"
  "v_cvt_pk_bf16_f32 v31, v94, v95\n"
  "v_mfma_f32_32x32x16_bf16 v[96:111], v[52:55], v[68:71], v[96:111]\n"
  "ds_read_b128 v[68:71], v2 offset:17408\n"
  "s_waitcnt lgkmcnt(2)\n"
  "v_mfma_f32_32x32x16_bf16 v[96:111], v[56:59], v[72:75], v[96:111]\n"
  "ds_read_b128 v[72:75], v2 offset:18432\n"
  "v_mfma_f32_32x32x16_bf16 v[96:111], v[60:63], v[76:79], v[96:111]\n"
  "ds_read_b128 v[76:79], v2 offset:19456\n"
  "s_waitcnt lgkmcnt(2)\n"
  "v_mfma_f32_32x32x16_bf16 v[80:95], v[32:35], v[64:67], 0\n"
  "ds_read_b128 v[64:67], v2 offset:20480\n"
  "v_mfma_f32_32x32x16_bf16 v[80:95], v[36:39], v[68:71], v[80:95]\n"
  "ds_read_b128 v[68:71], v2 offset:21504\n"
  "s_waitcnt lgkmcnt(2)\n"
  "v_mfma_f32_32x32x16_bf16 v[80:95], v[40:43], v[72:75], v[80:95]\n"
  "ds_read_b128 v[72:75], v2 offset:22528\n"
  "v_mfma_f32_32x32x16_bf16 v[80:95], v[44:47], v[76:79], v[80:95]\n"
  "ds_read_b128 v[76:79], v2 offset:23552\n"
  "s_waitcnt lgkmcnt(2)\n"
  "v_mfma_f32_32x32x16_bf16 v[80:95], v[48:51], v[64:67], v[80:95]\n"
  "ds_read_b128 v[64:67], v2 offset:24576\n"
  "v_mfma_f32_32x32x16_bf16 v[80:95], v[52:55], v[68:71], v[80:95]\n"
  "ds_read_b128 v[68:71], v2 offset:25600\n"
  "s_waitcnt lgkmcnt(2)\n"
  "v_mfma_f32_32x32x16_bf16 v[80:95], v[56:59], v[72:75], v[80:95]\n"
  "ds_read_b128 v[72:75], v2 offset:26624\n"
  "v_cvt_pk_bf16_f32 v112, v96, v97\n"
  "v_mfma_f32_32x32x16_bf16 v[80:95], v[60:63], v[76:79], v[80:95]\n"
  "ds_read_b128 v[76:79], v2 offset:27648\n"
  "v_cvt_pk_bf16_f32 v113, v98, v99\n"
  "v_cvt_pk_bf16_f32 v114, v100, v101\n"
  "v_cvt_pk_bf16_f32 v115, v102, v103\n"
  "v_cvt_pk_bf16_f32 v116, v104, v105\n"
  "v_cvt_pk_bf16_f32 v117, v106, v107\n"
  "v_cvt_pk_bf16_f32 v118, v108, v109\n"
  "v_cvt_pk_bf16_f32 v119, v110, v111\n"
  "s_waitcnt lgkmcnt(2)\n"
  "v_mfma_f32_32x32x16_bf16 v[96:111], v[32:35], v[64:67], 0\n"
  "ds_read_b128 v[64:67], v2 offset:28672\n"
  "v_mfma_f32_32x32x16_bf16 v[96:111], v[36:39], v[68:71], v[96:111]\n"
  "ds_read_b128 v[68:71], v2 offset:29696\n"
  "s_waitcnt lgkmcnt(2)\n"
  "v_mfma_f32_32x32x16_bf16 v[96:111], v[40:43], v[72:75], v[96:111]\n"
  "ds_read_b128 v[72:75], v2 offset:30720\n"
  "v_xor_b32 v9, 0xffff, v9\n"
  "v_and_b32 v3, v9, v10\n"
  "v_xor_b32 v9, v9, v3\n"
  "v_mfma_f32_32x32x16_bf16 v[96:111], v[44:47], v[76:79], v[96:111]\n"
  "ds_read_b128 v[76:79], v2 offset:31744\n"
  "v_cvt_pk_bf16_f32 v120, v80, v81\n"
  "v_cvt_pk_bf16_f32 v121, v82, v83\n"
  "v_cvt_pk_bf16_f32 v122, v84, v85\n"
  "v_cvt_pk_bf16_f32 v123, v86, v87\n"
  "s_waitcnt lgkmcnt(2)\n"
  "v_mfma_f32_32x32x16_bf16 v[96:111], v[48:51], v[64:67], v[96:111]\n"
  "v_cvt_pk_bf16_f32 v124, v88, v89\n"
  "v_cvt_pk_bf16_f32 v125, v90, v91\n"
  "v_cvt_pk_bf16_f32 v126, v92, v93\n"
  "v_cvt_pk_bf16_f32 v127, v94, v95\n"
  "v_mfma_f32_32x32x16_bf16 v[96:111], v[52:55], v[68:71], v[96:111]\n"
  "s_waitcnt lgkmcnt(0)\n"
  "v_mfma_f32_32x32x16_bf16 v[96:111], v[56:59], v[72:75], v[96:111]\n"
  "v_lshlrev_b32 v80, 16, v32\n"
  "v_and_b32 v81, 0xffff0000, v32\n"
  "v_lshlrev_b32 v82, 16, v33\n"
  "v_and_b32 v83, 0xffff0000, v33\n"
  "v_mfma_f32_32x32x16_bf16 v[96:111], v[60:63], v[76:79], v[96:111]\n"
  "v_lshlrev_b32 v84, 16, v34\n"
  "v_and_b32 v85, 0xffff0000, v34\n"
  "v_lshlrev_b32 v86, 16, v35\n"
  "v_and_b32 v87, 0xffff0000, v35\n"
  "v_lshlrev_b32 v88, 16, v36\n"
  "v_and_b32 v89, 0xffff0000, v36\n"
  "v_lshlrev_b32 v90, 16, v37\n"
  "v_and_b32 v91, 0xffff0000, v37\n"
  "v_lshlrev_b32 v92, 16, v38\n"
  "v_and_b32 v93, 0xffff0000, v38\n"
  "v_lshlrev_b32 v94, 16, v39\n"
  "v_and_b32 v95, 0xffff0000, v39\n"
  "s_waitcnt lgkmcnt(0)\n"
  ".Lat_w%=:\n"
  "ds_read_b32 v22, v4 offset:1032\n"
  "s_sub_u32 s60, s60, 1\n"
  "s_cbranch_scc1 .Lat_x%=\n"
  "s_waitcnt lgkmcnt(0)\n"
  "v_cmp_eq_u32 vcc, 0, v22\n"
  "s_cbranch_vccnz .Lat_w%=\n"
  ".Lat_x%=:\n"
  "ds_read_b64 v[20:21], v4 offset:1024\n"
  "ds_read_b128 v[12:15], v4\n"
  "s_waitcnt lgkmcnt(1)\n"
  "v_mov_b32 v1, v20\n"
  "v_mov_b32 v7, v21\n"
  "s_waitcnt lgkmcnt(0)\n"
  "v_and_b32 v16, v12, v9\n"
  "v_and_b32 v17, v12, v3\n"
  "v_and_b32 v18, v13, v9\n"
  "v_and_b32 v19, v13, v3\n"
  "v_and_b32 v20, v14, v9\n"
  "v_and_b32 v21, v14, v3\n"
  "v_and_b32 v22, v15, v9\n"
  "v_and_b32 v23, v15, v3\n"
  "s_nop 1\n"
  "s_branch .Lat_t%=\n"
  ".Lat_e%=:\n"
      : "+{v[0:15]}"(va), "+{v[16:23]}"(vb), "+{v[24:27]}"(mk), "+{v[48:51]}"(z4), "+{v[60:63]}"(A), "+{v[64:79]}"(ixa), "+{v[80:87]}"(ixb),
        "+{v[106:113]}"(vc), "+{v[124:125]}"(vd), "+{s[36:51]}"(sa)
      : "{s[52:59]}"(sb), "{s[68:75]}"(sc), [ow] "n"(O_W)
      : "memory", "vcc", "scc", CLOB);
}

extern "C" void kernel_launch(void* const* d_in, const int* in_sizes, int n_in, void* d_out, int out_size, void* d_ws, size_t ws_size,
                              hipStream_t stream) {
  char* ws = (char*)d_ws;
  (void)hipFuncSetAttribute((const void*)k_main, hipFuncAttributeMaxDynamicSharedMemorySize, LDS_MAIN);
  k_main<<<256, 1024, LDS_MAIN, stream>>>((const u16*)d_in[0], (const int*)d_in[1], (const int*)d_in[2], (const u16*)d_in[3], (const u16*)d_in[4],
                                          (const u16*)d_in[5], (const u16*)d_in[6], (const u16*)d_in[7], (const u16*)d_in[8], ws, (u16*)d_out);
}
```
